```python
import torch

def _softmax(x, axis=-1):
    return torch.softmax(x, dim=axis)
def _gelu(x):
    return torch.nn.functional.gelu(x, approximate="tanh")
def _silu(x):
    return torch.nn.functional.silu(x)
def _sigmoid(x):
    return torch.sigmoid(x)
def _layernorm(x, axis=-1, eps=1e-5):
    m = x.mean(dim=axis, keepdim=True)
    v = x.var(dim=axis, keepdim=True, unbiased=False)
    return (x - m) / torch.sqrt(v + eps)
def _scatter_add(tgt, idx, src, axis=0):
    return tgt.clone().index_add_(axis, idx, src)
def _segment_sum(src, seg, n):
    out = torch.zeros((n,) + src.shape[1:], dtype=src.dtype)
    return out.index_add_(0, seg, src)
def _pad(x, pw):
    flat = tuple(v for pair in reversed(pw) for v in pair)
    return torch.nn.functional.pad(x, flat)

kernel_name = 'interleave_layers__gqa_block_cached__mha_block_cached__decode'

def setup_inputs():

    _w = (lambda a: (lambda t: t.to(torch.bfloat16) if t.is_floating_point() else t)(torch.as_tensor(a)))
    def _A_setup():
        B, Sq, D = 8, 1, 512
        Hq, Hkv, Sk, Dh = 32, 8, 4096, 128
        assert Hq % Hkv == 0, (Hq, Hkv)
        return {
            "x": _w((lambda *s: torch.randn(*s, dtype=torch.bfloat16) * 0.1)(B, Sq, D)),
            "k_cache": _w((lambda *s: torch.randn(*s, dtype=torch.bfloat16) * 0.8)(B, Hkv, Sk, Dh)),
            "v_cache": _w((lambda *s: torch.randn(*s, dtype=torch.bfloat16) * 6.4)(B, Hkv, Sk, Dh)),
            "wq": _w((lambda *s: torch.randn(*s, dtype=torch.bfloat16) * 0.8)(D, Hq * Dh) * (D ** -0.5)),
            "wo": _w((lambda *s: torch.randn(*s, dtype=torch.bfloat16) * 6.4)(Hq * Dh, D) * ((Hq * Dh) ** -0.5)),
        }
    _A_in = _A_setup()

    def _B_setup():
        B, Sq, D = 8, 1, 512
        H, Sk, Dh = 32, 4096, 128
        return {
            "x": _w((lambda *s: torch.randn(*s, dtype=torch.bfloat16) * 0.1)(B, Sq, D)),
            "k_cache": _w((lambda *s: torch.randn(*s, dtype=torch.bfloat16) * 1.6)(B, H, Sk, Dh)),
            "v_cache": _w((lambda *s: torch.randn(*s, dtype=torch.bfloat16) * 3.2)(B, H, Sk, Dh)),
            "wq": _w((lambda *s: torch.randn(*s, dtype=torch.bfloat16) * 1.6)(D, H * Dh) * (D ** -0.5)),
            "wo": _w((lambda *s: torch.randn(*s, dtype=torch.bfloat16) * 3.2)(H * Dh, D) * ((H * Dh) ** -0.5)),
        }
    _B_in = _B_setup()

    return {
        "A_x": _A_in["x"],
        "A_k_cache": _A_in["k_cache"],
        "A_v_cache": _A_in["v_cache"],
        "A_wq": _A_in["wq"],
        "A_wo": _A_in["wo"],
        "B_x": _B_in["x"],
        "B_k_cache": _B_in["k_cache"],
        "B_v_cache": _B_in["v_cache"],
        "B_wq": _B_in["wq"],
        "B_wo": _B_in["wo"],
    }

def reference(A_x, A_k_cache, A_v_cache, A_wq, A_wo, B_x, B_k_cache, B_v_cache, B_wq, B_wo):
    def _A_ref(x, k_cache, v_cache, wq, wo):
        B, Sq, D = x.shape
        Hq, Dh = 32, 128
        Hkv = k_cache.shape[1]
        rep = Hq // Hkv
        q = (lambda x, *d: x.permute(*d))((x @ wq).reshape(B, Sq, Hq, Dh), 0, 2, 1, 3)
        k = (lambda x, n, axis: torch.repeat_interleave(x, n, dim=axis))(k_cache, rep, axis=1)
        v = (lambda x, n, axis: torch.repeat_interleave(x, n, dim=axis))(v_cache, rep, axis=1)
        scores = torch.einsum("bhqd,bhkd->bhqk", q, k) * (Dh ** -0.5)
        p = _softmax(scores, axis=-1)
        o = torch.einsum("bhqk,bhkd->bhqd", p, v)
        return (lambda x, *d: x.permute(*d))(o, 0, 2, 1, 3).reshape(B, Sq, Hq * Dh) @ wo

    def _B_ref(x, k_cache, v_cache, wq, wo):
        B, Sq, D = x.shape
        H, Dh = 32, 128
        q = (lambda x, *d: x.permute(*d))((x @ wq).reshape(B, Sq, H, Dh), 0, 2, 1, 3)
        scores = torch.einsum("bhqd,bhkd->bhqk", q, k_cache) * (Dh ** -0.5)
        p = _softmax(scores, axis=-1)
        o = torch.einsum("bhqk,bhkd->bhqd", p, v_cache)
        return (lambda x, *d: x.permute(*d))(o, 0, 2, 1, 3).reshape(B, Sq, H * Dh) @ wo

    x = A_x
    x = x + _A_ref(_layernorm(x, axis=-1), A_k_cache, A_v_cache, A_wq, A_wo)
    x = x + _B_ref(_layernorm(x, axis=-1), B_k_cache, B_v_cache, B_wq, B_wo)
    x = x + _A_ref(_layernorm(x, axis=-1), A_k_cache, A_v_cache, A_wq, A_wo)
    x = x + _B_ref(_layernorm(x, axis=-1), B_k_cache, B_v_cache, B_wq, B_wo)
    return x
```

```cpp
#include <hip/hip_runtime.h>
#include <hip/hip_bf16.h>
#include <stdint.h>

namespace v0 {
typedef unsigned v4u __attribute__((ext_vector_type(4)));
typedef float f32x4 __attribute__((ext_vector_type(4)));
typedef short s16x4 __attribute__((ext_vector_type(4)));
typedef __bf16 bf16x2 __attribute__((ext_vector_type(2)));
constexpr int NB = 8, DM = 512, NH = 32, SK = 4096, DH = 128;
constexpr float SCALE = 0.08838834764831845f;
constexpr float LOG2E = 1.4426950408889634f;

__device__ __forceinline__ float bf2f(unsigned short b) { return __builtin_bit_cast(float, (unsigned)b << 16); }
__device__ __forceinline__ unsigned short f2bf(float f) { return __builtin_bit_cast(unsigned short, (__bf16)f); }
__device__ __forceinline__ float rbf(float f) { return bf2f(f2bf(f)); }
__device__ __forceinline__ float dot2(unsigned a, unsigned b, float c) {
  return __builtin_amdgcn_fdot2_f32_bf16(__builtin_bit_cast(bf16x2, a), __builtin_bit_cast(bf16x2, b), c, false);
}
template <int CTRL> __device__ __forceinline__ float dppf(float x) {
  return __builtin_bit_cast(float, __builtin_amdgcn_mov_dpp(__builtin_bit_cast(int, x), CTRL, 0xf, 0xf, true));
}
template <int CTRL> __device__ __forceinline__ unsigned dppu(unsigned x) {
  return (unsigned)__builtin_amdgcn_mov_dpp((int)x, CTRL, 0xf, 0xf, true);
}
constexpr int XOR1 = 0xB1, XOR2 = 0x4E, XOR7 = 0x141, XOR8 = 0x128, ROR4 = 0x124;

__global__ void __launch_bounds__(128) k_qproj(const unsigned short* x, const unsigned short* wq, unsigned short* q) {
  __shared__ float xs[DM];
  __shared__ float red[4];
  const int h = blockIdx.x, b = blockIdx.y, t = threadIdx.x;
  float v[4], s = 0.f;
  for (int i = 0; i < 4; ++i) { v[i] = bf2f(x[b * DM + t + 128 * i]); s += v[i]; }
  for (int o = 32; o; o >>= 1) s += __shfl_xor(s, o);
  if ((t & 63) == 0) red[t >> 6] = s;
  __syncthreads();
  const float mean = (red[0] + red[1]) * (1.f / DM);
  float ss = 0.f;
  for (int i = 0; i < 4; ++i) { const float d = v[i] - mean; ss += d * d; }
  for (int o = 32; o; o >>= 1) ss += __shfl_xor(ss, o);
  if ((t & 63) == 0) red[2 + (t >> 6)] = ss;
  __syncthreads();
  const float mb = rbf(mean), vb = rbf((red[2] + red[3]) * (1.f / DM));
  const float den = rbf(sqrtf(rbf(vb + 1e-5f)));
  for (int i = 0; i < 4; ++i) xs[t + 128 * i] = rbf(rbf(v[i] - mb) / den);
  __syncthreads();
  float acc = 0.f;
  const unsigned short* wp = wq + h * DH + t;
#pragma unroll 8
  for (int k = 0; k < DM; ++k) acc = fmaf(xs[k], bf2f(wp[(size_t)k * (NH * DH)]), acc);
  q[(b * NH + h) * DH + t] = f2bf(acc);
}

template <int G, int NSPLIT>
__global__ void __launch_bounds__(256) k_attn(const unsigned short* q, const unsigned short* kc, const unsigned short* vc, float* part) {
  constexpr int HKV = NH / G, KPW = SK / NSPLIT / 4, NCH = KPW / 64;
  const int sp = blockIdx.x, kvh = blockIdx.y, b = blockIdx.z;
  const int lane = threadIdx.x & 63, w = threadIdx.x >> 6, g = lane >> 4, r = lane & 15;
  const size_t base = ((size_t)(b * HKV + kvh) * SK + (size_t)(sp * 4 + w) * KPW) * DH;
  const v4u* kp = (const v4u*)(kc + base) + lane;
  const v4u* vp = (const v4u*)(vc + base) + lane;
  v4u qv[G];
#pragma unroll
  for (int h = 0; h < G; ++h) qv[h] = *(const v4u*)(q + (size_t)(b * NH + kvh * G + h) * DH + 8 * r);
  const unsigned m0 = (r & 3) == 0 ? 0xFFFFu : (r & 3) == 1 ? 0xFFFF0000u : 0u;
  const unsigned m1 = (r & 3) == 2 ? 0xFFFFu : (r & 3) == 3 ? 0xFFFF0000u : 0u;
  float m[G], l[G];
  f32x4 acc[G][2];
#pragma unroll
  for (int h = 0; h < G; ++h) { m[h] = -INFINITY; l[h] = 0.f; acc[h][0] = f32x4{0, 0, 0, 0}; acc[h][1] = f32x4{0, 0, 0, 0}; }
  for (int ch = 0; ch < NCH; ++ch) {
    v4u K[16], V[16];
#pragma unroll
    for (int u = 0; u < 16; ++u) K[u] = __builtin_nontemporal_load(kp + (size_t)(ch * 16 + u) * 64);
#pragma unroll
    for (int u = 0; u < 16; ++u) V[u] = __builtin_nontemporal_load(vp + (size_t)(ch * 16 + u) * 64);
    __builtin_amdgcn_sched_barrier(0);
#pragma unroll
    for (int h = 0; h < G; ++h) {
      float t[16];
#pragma unroll
      for (int u = 0; u < 16; ++u) t[u] = dot2(K[u].w, qv[h].w, dot2(K[u].z, qv[h].z, dot2(K[u].y, qv[h].y, dot2(K[u].x, qv[h].x, 0.f))));
#define TR_STEP(HALF, CTRL, BIT) _Pragma("unroll") for (int i = 0; i < (HALF); ++i) { \
      const float lo_ = t[i], hi_ = t[i + (HALF)]; const float a_ = lo_ + dppf<CTRL>(lo_), b_ = hi_ + dppf<CTRL>(hi_); t[i] = (r & (BIT)) ? b_ : a_; }
      TR_STEP(8, XOR8, 8) TR_STEP(4, XOR7, 4) TR_STEP(2, XOR2, 2) TR_STEP(1, XOR1, 1)
#undef TR_STEP
      const float s = rbf(rbf(t[0]) * SCALE);
      float mx = s;
      for (int o = 32; o; o >>= 1) mx = fmaxf(mx, __shfl_xor(mx, o));
      const float mn = fmaxf(m[h], mx);
      const float alpha = __builtin_amdgcn_exp2f((m[h] - mn) * LOG2E);
      const float p = __builtin_amdgcn_exp2f((s - mn) * LOG2E);
      l[h] = l[h] * alpha + p;
      m[h] = mn;
      acc[h][0] *= alpha; acc[h][1] *= alpha;
      const unsigned pb = f2bf(p), pp = pb | (pb << 16);
#define PV(U) { const unsigned bc = dppu<0x150 + (U)>(pp); const unsigned b0 = bc & m0, b1 = bc & m1; \
        typedef unsigned v2u __attribute__((ext_vector_type(2))); \
        const s16x4 bo = __builtin_bit_cast(s16x4, v2u{b0, b1}); \
        acc[h][0] = __builtin_amdgcn_mfma_f32_16x16x16bf16_1k(__builtin_bit_cast(s16x4, v2u{V[U].x, V[U].y}), bo, acc[h][0], 0, 0, 0); \
        acc[h][1] = __builtin_amdgcn_mfma_f32_16x16x16bf16_1k(__builtin_bit_cast(s16x4, v2u{V[U].z, V[U].w}), bo, acc[h][1], 0, 0, 0); }
      PV(0) PV(1) PV(2) PV(3) PV(4) PV(5) PV(6) PV(7) PV(8) PV(9) PV(10) PV(11) PV(12) PV(13) PV(14) PV(15)
#undef PV
    }
  }
#pragma unroll
  for (int h = 0; h < G; ++h) {
    float ls = l[h];
    for (int o = 32; o; o >>= 1) ls += __shfl_xor(ls, o);
    float* rec = part + ((size_t)(b * NH + kvh * G + h) * (NSPLIT * 4) + sp * 4 + w) * 132;
    if (r < 4) {
#pragma unroll
      for (int i = 0; i < 4; ++i) { rec[8 * (4 * g + i) + r] = acc[h][0][i]; rec[8 * (4 * g + i) + 4 + r] = acc[h][1][i]; }
    }
    if (lane == 0) { rec[128] = m[h]; rec[129] = ls; }
  }
}

template <int NP>
__global__ void __launch_bounds__(256) k_out(const float* part, const unsigned short* wo, const unsigned short* xin, unsigned short* xout) {
  __shared__ float os[NH * DH];
  __shared__ float wt[NH][NP];
  __shared__ float red[16][17];
  const int cg = blockIdx.x, b = blockIdx.y, t = threadIdx.x;
  if (t < NH) {
    const float* rec = part + (size_t)(b * NH + t) * NP * 132;
    float M = -INFINITY;
    for (int s = 0; s < NP; ++s) M = fmaxf(M, rec[s * 132 + 128]);
    float L = 0.f;
    for (int s = 0; s < NP; ++s) { const float e = __builtin_amdgcn_exp2f((rec[s * 132 + 128] - M) * LOG2E); wt[t][s] = e; L += e * rec[s * 132 + 129]; }
    const float inv = 1.f / L;
    for (int s = 0; s < NP; ++s) wt[t][s] *= inv;
  }
  __syncthreads();
  for (int i = t; i < NH * DH; i += 256) {
    const int h = i >> 7, d = i & 127;
    const float* rec = part + (size_t)(b * NH + h) * NP * 132 + d;
    float a = 0.f;
#pragma unroll
    for (int s = 0; s < NP; ++s) a = fmaf(wt[h][s], rec[s * 132], a);
    os[i] = rbf(a);
  }
  __syncthreads();
  const int c = t & 15, kg = t >> 4;
  float acc = 0.f;
  const unsigned short* wp = wo + cg * 16 + c;
#pragma unroll 8
  for (int k = kg; k < NH * DH; k += 16) acc = fmaf(os[k], bf2f(wp[(size_t)k * DM]), acc);
  red[kg][c] = acc;
  __syncthreads();
  if (t < 16) {
    float s = 0.f;
    for (int i = 0; i < 16; ++i) s += red[i][t];
    const int idx = b * DM + cg * 16 + t;
    xout[idx] = f2bf(bf2f(xin[idx]) + rbf(s));
  }
}
}

extern "C" void kernel_launch(void* const* d_in, const int* in_sizes, int n_in, void* d_out, int out_size, void* d_ws, size_t ws_size, hipStream_t stream) {
  using namespace v0;
  const unsigned short* x0 = (const unsigned short*)d_in[0];
  const unsigned short *kA = (const unsigned short*)d_in[1], *vA = (const unsigned short*)d_in[2], *wqA = (const unsigned short*)d_in[3], *woA = (const unsigned short*)d_in[4];
  const unsigned short *kB = (const unsigned short*)d_in[6], *vB = (const unsigned short*)d_in[7], *wqB = (const unsigned short*)d_in[8], *woB = (const unsigned short*)d_in[9];
  unsigned char* ws = (unsigned char*)d_ws;
  unsigned short* q = (unsigned short*)ws;
  unsigned short* xa = (unsigned short*)(ws + 65536);
  unsigned short* xb = (unsigned short*)(ws + 65536 + 8192);
  float* part = (float*)(ws + (1 << 20));
  const unsigned short* xin = x0;
  for (int layer = 0; layer < 4; ++layer) {
    unsigned short* xo = layer == 3 ? (unsigned short*)d_out : (layer & 1) ? xb : xa;
    if ((layer & 1) == 0) {
      k_qproj<<<dim3(NH, NB), 128, 0, stream>>>(xin, wqA, q);
      k_attn<4, 8><<<dim3(8, 8, NB), 256, 0, stream>>>(q, kA, vA, part);
      k_out<32><<<dim3(DM / 16, NB), 256, 0, stream>>>(part, woA, xin, xo);
    } else {
      k_qproj<<<dim3(NH, NB), 128, 0, stream>>>(xin, wqB, q);
      k_attn<1, 2><<<dim3(2, NH, NB), 256, 0, stream>>>(q, kB, vB, part);
      k_out<8><<<dim3(DM / 16, NB), 256, 0, stream>>>(part, woB, xin, xo);
    }
    xin = xo;
  }
}
```

```cpp
#include <hip/hip_runtime.h>
#include <hip/hip_bf16.h>
#include <stdint.h>

namespace mk {
typedef unsigned v4u __attribute__((ext_vector_type(4)));
typedef unsigned v2u __attribute__((ext_vector_type(2)));
typedef float f32x4 __attribute__((ext_vector_type(4)));
typedef float f32x2 __attribute__((ext_vector_type(2)));
typedef short s16x4 __attribute__((ext_vector_type(4)));
typedef __bf16 bf16x8 __attribute__((ext_vector_type(8)));
typedef __bf16 bf16x2 __attribute__((ext_vector_type(2)));
typedef __amdgpu_buffer_rsrc_t rsrc_t;
#define LAS __attribute__((address_space(3)))

constexpr int NB = 8, DM = 512, NH = 32, HKV = 8, SK = 4096, DH = 128;
constexpr float SCALE = 0.08838834764831845f;
constexpr float LOG2E = 1.4426950408889634f;
constexpr int NWG = 256, NTHR = 512;
constexpr int NSRC = 28;
constexpr int REC_B = 640, HEAD_B = NSRC * REC_B;
constexpr unsigned SENT = 0xFFFFFFFFu;
constexpr unsigned SPIN_CAP = 1u << 16;
constexpr unsigned WS_REC = 0, WS_Y = NB * 4 * NH * HEAD_B, WS_Q = WS_Y + NB * 4 * NH * DM * 4, WS_END = WS_Q + NB * 4 * NH * DH * 2;
constexpr unsigned WS_DBG = 32u << 20;
constexpr int NS = 12;
constexpr int NPMAX = 19, NO = NPMAX - NS;
#define MK_THR 6
#define MK_THR_LO 4
#define MK_NLO 4
#define MK_THR_HI 8
#define MK_THR_A 4
constexpr int THR = MK_THR;
static_assert(THR <= 6, "the GQA pass relies on six younger loads behind its LDS fills");
constexpr int A_BYTES = 147 * 512, BOX = 2080;
constexpr int L_SCR = 2 * A_BYTES, L_QB = L_SCR + 4 * BOX, L_FLG = L_QB + 2 * 1280, L_STAMP = L_FLG + 256, LDS_BYTES = 160 * 1024;
constexpr int S_W = 0, S_XS = 4 * 32768, S_RED = S_XS + NB * DM * 2, S_RED2 = S_RED + 8 * DM * 4, S_ST = S_RED2 + 8 * DH * 4;
static_assert(S_ST + 256 <= L_STAMP && L_STAMP + 2048 <= LDS_BYTES, "LDS map");

struct Params {
  const unsigned short *x0, *kA, *vA, *wqA, *woA, *kB, *vB, *wqB, *woB;
  unsigned short* out;
  unsigned char* ws;
};

__device__ __forceinline__ float bf2f(unsigned b) { return __builtin_bit_cast(float, b << 16); }
__device__ __forceinline__ unsigned f2bf(float f) { return (unsigned)__builtin_bit_cast(unsigned short, (__bf16)f); }
__device__ __forceinline__ float rbf(float f) { return bf2f(f2bf(f)); }
__device__ __forceinline__ unsigned pk2(float lo, float hi) { return __builtin_bit_cast(unsigned, bf16x2{(__bf16)lo, (__bf16)hi}); }
__device__ __forceinline__ float dot2(unsigned a, unsigned b, float c) {
  return __builtin_amdgcn_fdot2_f32_bf16(__builtin_bit_cast(bf16x2, a), __builtin_bit_cast(bf16x2, b), c, false);
}
template <int CTRL> __device__ __forceinline__ float dppf(float x) {
  return __builtin_bit_cast(float, __builtin_amdgcn_mov_dpp(__builtin_bit_cast(int, x), CTRL, 0xf, 0xf, true));
}
template <int CTRL> __device__ __forceinline__ unsigned dppu(unsigned x) { return (unsigned)__builtin_amdgcn_mov_dpp((int)x, CTRL, 0xf, 0xf, true); }
constexpr int XOR1 = 0xB1, XOR2 = 0x4E, XOR7 = 0x141, XOR8 = 0x128, ROR4 = 0x124, BCAST = 0x150;
__device__ __forceinline__ float fmx(float a, float b) { return __builtin_fmaxf(a, b); }
__device__ __forceinline__ float xrow_max(float x) {
  auto s = __builtin_amdgcn_permlane16_swap(__float_as_uint(x), __float_as_uint(x), false, false);
  x = fmx(__uint_as_float(s[0]), __uint_as_float(s[1]));
  auto t = __builtin_amdgcn_permlane32_swap(__float_as_uint(x), __float_as_uint(x), false, false);
  return fmx(__uint_as_float(t[0]), __uint_as_float(t[1]));
}
__device__ __forceinline__ float xrow_sum(float x) {
  auto s = __builtin_amdgcn_permlane16_swap(__float_as_uint(x), __float_as_uint(x), false, false);
  x = __uint_as_float(s[0]) + __uint_as_float(s[1]);
  auto t = __builtin_amdgcn_permlane32_swap(__float_as_uint(x), __float_as_uint(x), false, false);
  return __uint_as_float(t[0]) + __uint_as_float(t[1]);
}
__device__ __forceinline__ float rs4(float a, float b, float c, float d) {
  auto s = __builtin_amdgcn_permlane16_swap(__float_as_uint(a), __float_as_uint(b), false, false);
  auto t = __builtin_amdgcn_permlane16_swap(__float_as_uint(c), __float_as_uint(d), false, false);
  const float u = __uint_as_float(s[0]) + __uint_as_float(s[1]), v = __uint_as_float(t[0]) + __uint_as_float(t[1]);
  auto r = __builtin_amdgcn_permlane32_swap(__float_as_uint(u), __float_as_uint(v), false, false);
  return __uint_as_float(r[0]) + __uint_as_float(r[1]);
}
__device__ __forceinline__ float row_max(float x) {
  x = fmx(x, dppf<XOR1>(x)); x = fmx(x, dppf<XOR2>(x)); x = fmx(x, dppf<ROR4>(x)); return fmx(x, dppf<XOR8>(x));
}
__device__ __forceinline__ float row_sum(float x) {
  x += dppf<XOR1>(x); x += dppf<XOR2>(x); x += dppf<ROR4>(x); return x + dppf<XOR8>(x);
}
__device__ __forceinline__ float ex2(float x) { return __builtin_amdgcn_exp2f(x); }
__device__ __forceinline__ rsrc_t mkrs(const void* p, unsigned bytes) { return __builtin_amdgcn_make_buffer_rsrc((void*)p, 0, (int)bytes, 0x00020000); }
__device__ __forceinline__ v4u ld_nt(rsrc_t rs, unsigned voff, unsigned soff) { return __builtin_amdgcn_raw_buffer_load_b128(rs, voff, soff, 2); }
__device__ __forceinline__ v4u ld_sc1(rsrc_t rs, unsigned voff, unsigned soff) { return __builtin_amdgcn_raw_buffer_load_b128(rs, voff, soff, 16); }
__device__ __forceinline__ v2u ld_sc1_64(rsrc_t rs, unsigned voff, unsigned soff) { return __builtin_amdgcn_raw_buffer_load_b64(rs, voff, soff, 16); }
__device__ __forceinline__ unsigned nosent(unsigned x) { return x < 0xFFFFFFFEu ? x : 0xFFFFFFFEu; }
__device__ __forceinline__ void st_sc1(rsrc_t rs, unsigned voff, unsigned soff, v4u v) {
  v.x = nosent(v.x); v.y = nosent(v.y); v.z = nosent(v.z); v.w = nosent(v.w);
  __builtin_amdgcn_raw_buffer_store_b128(v, rs, voff + soff, 0, 16);
}
__device__ __forceinline__ void st_sc1_64(rsrc_t rs, unsigned voff, unsigned soff, v2u v) {
  v.x = nosent(v.x); v.y = nosent(v.y);
  __builtin_amdgcn_raw_buffer_store_b64(v, rs, voff, soff, 16);
}
__device__ __forceinline__ void st_sc1_32(rsrc_t rs, unsigned voff, unsigned soff, unsigned v) { __builtin_amdgcn_raw_buffer_store_b32(nosent(v), rs, voff, soff, 16); }
__device__ __forceinline__ unsigned umax4(v4u v) { return max(max(v.x, v.y), max(v.z, v.w)); }
__device__ __forceinline__ bool all_lanes(bool ok) { return __builtin_amdgcn_ballot_w64(!ok) == 0ull; }
#define CFENCE() asm volatile("" ::: "memory")
__device__ __forceinline__ int lane_id() { return (int)__builtin_amdgcn_mbcnt_hi(~0u, __builtin_amdgcn_mbcnt_lo(~0u, 0u)); }
__device__ __forceinline__ int opaque(int v) { asm volatile("" : "+v"(v)); return v; }
#define SCHED0() __builtin_amdgcn_sched_barrier(0)
template <int N> __device__ __forceinline__ void wait_vm() { asm volatile("s_waitcnt vmcnt(%0)" ::"n"(N) : "memory"); }
__device__ __forceinline__ void bar() { asm volatile("s_waitcnt lgkmcnt(0)\n\ts_barrier" ::: "memory"); }
__device__ __forceinline__ void wait_lgkm() { asm volatile("s_waitcnt lgkmcnt(0)" ::: "memory"); }
__device__ __forceinline__ f32x4 mfma16(unsigned a0, unsigned a1, unsigned b0, unsigned b1, f32x4 c) {
  return __builtin_amdgcn_mfma_f32_16x16x16bf16_1k(__builtin_bit_cast(s16x4, v2u{a0, a1}), __builtin_bit_cast(s16x4, v2u{b0, b1}), c, 0, 0, 0);
}
__device__ __forceinline__ f32x4 mfma32(v4u a, v4u b, f32x4 c) {
  return __builtin_amdgcn_mfma_f32_16x16x32_bf16(__builtin_bit_cast(bf16x8, a), __builtin_bit_cast(bf16x8, b), c, 0, 0, 0);
}
__device__ __forceinline__ void lds_wait_ge(volatile LAS unsigned* f, unsigned v) {
  for (unsigned spins = 0; *f < v && spins < (SPIN_CAP << 7); ++spins) __builtin_amdgcn_s_sleep(1);
  CFENCE();
}
__device__ __forceinline__ void lds_wait_ge_sleep(volatile LAS unsigned* f, unsigned v) {
  for (unsigned spins = 0; *f < v && spins < (SPIN_CAP << 6); ++spins) __builtin_amdgcn_s_sleep(2);
  CFENCE();
}
__device__ __forceinline__ void lds_wait_ge4(volatile LAS v4u* f, unsigned v) {
  for (unsigned spins = 0; spins < (SPIN_CAP << 8); ++spins) {
    const v4u t = *f;
    if (min(min(t.x, t.y), min(t.z, t.w)) >= v) break;
  }
  CFENCE();
}

struct SoftState { float m, l; f32x4 a; };

template <int U0, int NU>
__device__ __forceinline__ float b_scores(const v4u (&K)[2 * NS], const v4u q, int r) {
  constexpr int P2 = NU > 8 ? 16 : NU > 4 ? 8 : NU > 2 ? 4 : 2;
  float t[16];
#pragma unroll
  for (int u = 0; u < NU; ++u) {
    const v4u k = K[(U0 + u) % (2 * NS)];
    t[u] = dot2(k.w, q.w, dot2(k.z, q.z, dot2(k.y, q.y, dot2(k.x, q.x, 0.f))));
  }
#define TR_STEP(HALF, CTRL, CNT)                                                                  \
  _Pragma("unroll") for (int i = 0; i < (HALF); ++i) {                                            \
    if (i < (CNT)) {                                                                              \
      const float a_ = t[i] + dppf<CTRL>(t[i]);                                                   \
      if (i + (HALF) < (CNT)) { const float b_ = t[i + (HALF)] + dppf<CTRL>(t[i + (HALF)]); t[i] = (r & (HALF)) ? b_ : a_; } \
      else t[i] = a_;                                                                             \
    }                                                                                             \
  }
  if constexpr (P2 >= 16) { TR_STEP(8, XOR8, NU) }
  if constexpr (P2 >= 8) { TR_STEP(4, XOR7, (NU < 8 ? NU : 8)) }
  if constexpr (P2 >= 4) { TR_STEP(2, XOR2, (NU < 4 ? NU : 4)) }
  TR_STEP(1, XOR1, 2)
#undef TR_STEP
  float x = t[0];
  if constexpr (P2 < 4) x += dppf<XOR2>(x);
  if constexpr (P2 < 8) x += dppf<ROR4>(x);
  if constexpr (P2 < 16) x += dppf<XOR8>(x);
  return x;
}
template <int U0, int NU>
__device__ __forceinline__ f32x4 b_pv(const v4u (&V)[2 * NS], unsigned pp, const v4u mk, f32x4 a) {
#define PV(U)                                                                                     \
  if constexpr ((U) < NU) {                                                                       \
    const unsigned bc = dppu<BCAST + (U)>(pp);                                                    \
    a = mfma32(V[(U0 + (U)) % (2 * NS)], v4u{bc & mk.x, bc & mk.y, bc & mk.z, bc & mk.w}, a);     \
  }
  PV(0) PV(1) PV(2) PV(3) PV(4) PV(5) PV(6) PV(7) PV(8) PV(9) PV(10) PV(11) PV(12) PV(13) PV(14) PV(15)
#undef PV
  return a;
}
template <int U0, int NU, bool FIRST>
__device__ __forceinline__ void b_chunk(const v4u (&K)[2 * NS], const v4u (&V)[2 * NS], const v4u q, bool ok0, SoftState& st, int r, const v4u mk) {
  constexpr int N0 = NU > 16 ? 16 : NU, N1 = NU - N0;
  const float x0 = b_scores<U0, N0>(K, q, r);
  const float s0 = (r < N0 && ok0) ? rbf(rbf(x0) * SCALE) : -INFINITY;
  float s1 = -INFINITY;
  if constexpr (N1 > 0) {
    const float x1 = b_scores<U0 + 16, N1>(K, q, r);
    s1 = r < N1 ? rbf(rbf(x1) * SCALE) : -INFINITY;
  }
  float mn = xrow_max(row_max(fmx(s0, s1)));
  if constexpr (!FIRST) mn = fmx(mn, st.m);
  const float p0 = ex2((s0 - mn) * LOG2E);
  if constexpr (FIRST) {
    st.l = p0;
    st.a = f32x4{0.f, 0.f, 0.f, 0.f};
  } else {
    const float alpha = ex2((st.m - mn) * LOG2E);
    st.l = st.l * alpha + p0;
    st.a *= alpha;
  }
  st.m = mn;
  st.a = b_pv<U0, N0>(V, pk2(p0, p0), mk, st.a);
  if constexpr (N1 > 0) {
    const float p1 = ex2((s1 - mn) * LOG2E);
    st.l += p1;
    st.a = b_pv<U0 + 16, N1>(V, pk2(p1, p1), mk, st.a);
  }
}
__device__ __forceinline__ v4u pv_masks(int lane) {
  const unsigned h = (lane & 1) ? 0xFFFF0000u : 0xFFFFu;
  const int t = (lane >> 1) & 3;
  return v4u{t == 0 ? h : 0u, t == 1 ? h : 0u, t == 2 ? h : 0u, t == 3 ? h : 0u};
}

__device__ __forceinline__ void b_publish(rsrc_t rw, unsigned head_off, int src, const SoftState& st, int lane) {
  const float l = xrow_sum(row_sum(st.l));
  const int c = lane & 15, lg = lane >> 4;
  const f32x4 ml = {st.m, l, 0.f, 0.f};
  const f32x4 v = c < 8 ? st.a : ml;
  const unsigned off = (unsigned)(src * REC_B) + (c < 8 ? (unsigned)((lg * 8 + c) * 16) : 512u);
  if (c < 8 || lane == 8) st_sc1(rw, off, head_off, __builtin_bit_cast(v4u, v));
}

template <int SLOT, int TH>
__device__ __forceinline__ void issue_pair(v4u (&K)[2 * NS], v4u (&V)[2 * NS], rsrc_t rk, rsrc_t rv, unsigned va, unsigned vb, unsigned soff) {
  wait_vm<TH>();
  K[2 * SLOT] = ld_nt(rk, va, soff);
  K[2 * SLOT + 1] = ld_nt(rk, vb + 1024, soff);
  SCHED0();
  wait_vm<TH>();
  V[2 * SLOT] = ld_nt(rv, va, soff);
  V[2 * SLOT + 1] = ld_nt(rv, vb + 1024, soff);
  SCHED0();
}

struct StreamCtx {
  rsrc_t rw;
  v4u rwv;
  LAS unsigned char* L;
  int lane, grp, wq, x, head, c28, widx;
  int nk;
  unsigned ldsA;
  unsigned a_off;
};
__device__ __forceinline__ volatile LAS unsigned* flags(const StreamCtx& c) { return (volatile LAS unsigned*)(c.L + L_FLG); }
__device__ __forceinline__ unsigned a_region(int grp, int c28, int wq) {
  return (unsigned)(grp * A_BYTES + (((c28 * 4 + wq) * 4096) / 112 - (c28 * 16384) / 112) * 512);
}

__device__ __forceinline__ void a_issue(const Params& p, const StreamCtx& c, int b) {
  const int lane = opaque(c.lane), a = lane >> 4, pos = lane & 15;
  const rsrc_t rk = mkrs(p.kA, NB * HKV * SK * DH * 2), rv = mkrs(p.vA, NB * HKV * SK * DH * 2);
  const unsigned so = (unsigned)b * (HKV * SK * DH * 2) + c.a_off;
  const unsigned kv = a * 256 + ((pos ^ (4 * a)) * 16), vv = lane * 16;
  LAS unsigned char* kd = c.L + c.ldsA;
  LAS unsigned char* vd = kd + c.nk * 256;
  if (c.nk == 37 && lane < 16) {
    __builtin_amdgcn_raw_ptr_buffer_load_lds(rk, (LAS void*)(kd + 9 * 1024), 16, kv ^ 16, so + 9 * 1024, 0, 2);
    __builtin_amdgcn_raw_ptr_buffer_load_lds(rv, (LAS void*)(vd + 9 * 1024), 16, vv, so + 9 * 1024, 0, 2);
  }
#pragma unroll
  for (int u = 0; u < 9; ++u) {
    wait_vm<MK_THR_A>();
    __builtin_amdgcn_raw_ptr_buffer_load_lds(rk, (LAS void*)(kd + u * 1024), 16, kv ^ ((u & 3) * 16), so + u * 1024, 0, 2);
    __builtin_amdgcn_raw_ptr_buffer_load_lds(rv, (LAS void*)(vd + u * 1024), 16, vv, so + u * 1024, 0, 2);
  }
}

template <bool SHARED>
__device__ __forceinline__ void a_merge(const StreamCtx& c, int b, int layer) {
  const int lane = opaque(c.lane), kg = lane >> 4, col = lane & 15, wq = c.wq;
  unsigned src;
  if constexpr (SHARED) src = (unsigned)(L_SCR + kg * BOX);
  else {
    const unsigned r0 = a_region(c.grp, c.c28, 0), r1 = a_region(c.grp, c.c28, 1), r2 = a_region(c.grp, c.c28, 2), r3 = a_region(c.grp, c.c28, 3);
    src = (kg & 2) ? ((kg & 1) ? r3 : r2) : ((kg & 1) ? r1 : r0);
  }
  const LAS unsigned char* in = c.L + src;
  const f32x2 ml = *(const LAS f32x2*)(in + 2048 + wq * 8);
  f32x4 b0 = *(const LAS f32x4*)(in + (wq * 16 + col) * 32), b1 = *(const LAS f32x4*)(in + (wq * 16 + col) * 32 + 16);
  const float M = xrow_max(ml.x), e = ex2((ml.x - M) * LOG2E);
  b0 *= e; b1 *= e;
  const float Lt = xrow_sum(ml.y * e);
  const float o0 = rs4(b0[0], b0[1], b0[2], b0[3]), o1 = rs4(b1[0], b1[1], b1[2], b1[3]);
  const unsigned ho = WS_REC + (unsigned)(((b * 4 + layer) * NH + 4 * c.x + wq) * HEAD_B);
  st_sc1_64(c.rw, (unsigned)(c.c28 * REC_B + col * 32 + kg * 8), ho, v2u{__float_as_uint(o0), __float_as_uint(o1)});
  if (lane == 0) st_sc1_64(c.rw, (unsigned)(c.c28 * REC_B + 512), ho, v2u{__float_as_uint(M), __float_as_uint(Lt)});
  CFENCE();
}
template <int NBYTES>
__device__ __forceinline__ void q_dma(const StreamCtx& c, unsigned qoff) {
  const unsigned dst = (unsigned)(size_t)(c.L + L_QB + c.grp * 1280 + (NBYTES == 1024 ? 0 : 1024));
  const unsigned voff = opaque(c.lane) * 16;
  if (voff < NBYTES) {
    unsigned keep;
    asm volatile("s_mov_b32 %0, m0\n\ts_mov_b32 m0, %1\n\ts_nop 0\n\tbuffer_load_dwordx4 %2, %3, %4 offen sc1 lds\n\ts_mov_b32 m0, %0"
                 : "=&s"(keep) : "s"(dst), "v"(voff), "s"(c.rwv), "s"(qoff) : "memory");
  }
}
template <int NBYTES>
__device__ __forceinline__ void q_arm(const StreamCtx& c, unsigned qoff, unsigned seq) {
  if (c.wq == 0) {
    const int lane = opaque(c.lane);
    *(LAS v4u*)(c.L + L_QB + c.grp * 1280 + (NBYTES == 1024 ? 0 : 1024) + ((lane * 16) & (NBYTES - 1))) = v4u{SENT, SENT, SENT, SENT};
    wait_lgkm();
    if (lane == 0) flags(c)[12 + c.grp] = seq;
    q_dma<NBYTES>(c, qoff);
  }
}
template <int NBYTES>
__device__ __forceinline__ void q_wait(const StreamCtx& c, unsigned qoff, unsigned seq) {
  const int lane = opaque(c.lane);
  volatile LAS v4u* qp = (volatile LAS v4u*)(c.L + L_QB + c.grp * 1280 + (NBYTES == 1024 ? 0 : 1024) + ((lane * 16) & (NBYTES - 1)));
  volatile LAS unsigned* armed = flags(c) + 12 + c.grp;
  for (unsigned spins = 0; spins < (SPIN_CAP << 6); ++spins) {
    const unsigned a = *armed;
    const v4u q = *qp;
    if (all_lanes(a >= seq && umax4(q) != SENT)) break;
    if (c.wq == 0 && (spins & 1) == 1) q_dma<NBYTES>(c, qoff);
    __builtin_amdgcn_s_sleep(1);
  }
  CFENCE();
}

struct AScores { float s[10]; };
__device__ __forceinline__ AScores a_qk(const StreamCtx& c) {
  const int lane = opaque(c.lane), kg = lane >> 4, col = lane & 15, hh = col >> 2;
  const LAS unsigned char* base = c.L + c.ldsA;
  const LAS unsigned char* qb = c.L + L_QB + c.grp * 1280 + hh * 256 + kg * 16;
  f32x4 sc[3] = {{0.f, 0.f, 0.f, 0.f}, {0.f, 0.f, 0.f, 0.f}, {0.f, 0.f, 0.f, 0.f}};
  const unsigned ro = (unsigned)((col & 3) * 1024 + (col >> 2) * 256);
#pragma unroll
  for (int th = 0; th < 2; ++th) {
    v4u qf[2], kf[2][3];
#pragma unroll
    for (int t2 = 0; t2 < 2; ++t2) {
      const int t = 2 * th + t2;
      const LAS unsigned char* kp = base + ro + 16 * ((4 * t + kg) ^ col);
      qf[t2] = *(const LAS v4u*)(qb + t * 64);
#pragma unroll
      for (int tau = 0; tau < 3; ++tau) kf[t2][tau] = *(const LAS v4u*)(kp + tau * 4096);
    }
    SCHED0();
#pragma unroll
    for (int t2 = 0; t2 < 2; ++t2)
#pragma unroll
      for (int tau = 0; tau < 3; ++tau) sc[tau] = mfma32(kf[t2][tau], qf[t2], sc[tau]);
  }
  AScores o;
#pragma unroll
  for (int u = 0; u < 10; ++u) o.s[u] = rbf(rbf(sc[u >> 2][u & 3]) * SCALE);
  if (!(c.nk == 37 && kg == 0)) o.s[9] = -INFINITY;
  return o;
}
__device__ __forceinline__ void a_pv(const StreamCtx& c, const AScores& sc, unsigned box) {
  const int lane = opaque(c.lane), kg = lane >> 4, col = lane & 15, hh = col >> 2;
  const unsigned m0 = (lane & 3) == 0 ? 0xFFFFu : (lane & 3) == 1 ? 0xFFFF0000u : 0u;
  const unsigned m1 = (lane & 3) == 2 ? 0xFFFFu : (lane & 3) == 3 ? 0xFFFF0000u : 0u;
  const LAS unsigned char* vbase = c.L + c.ldsA + c.nk * 256 + lane * 16;
  float mx = sc.s[0];
#pragma unroll
  for (int u = 1; u < 10; ++u) mx = fmx(mx, sc.s[u]);
  const float m = xrow_max(mx), mneg = -m * LOG2E;
  float l = 0.f;
  f32x4 a0 = {0.f, 0.f, 0.f, 0.f}, a1 = {0.f, 0.f, 0.f, 0.f};
#pragma unroll
  for (int u = 0; u < 10; ++u) {
    if (u < 9 || c.nk == 37) {
      const float pr = ex2(fmaf(sc.s[u], LOG2E, mneg));
      l += pr;
      const unsigned pp = pk2(pr, pr), b0 = pp & m0, b1 = pp & m1;
      v4u v = *(const LAS v4u*)(vbase + u * 1024);
      if (u == 9 && kg != 0) v = v4u{0u, 0u, 0u, 0u};
      a0 = mfma16(v.x, v.y, b0, b1, a0);
      a1 = mfma16(v.z, v.w, b0, b1, a1);
    }
  }
  l = xrow_sum(l);
  LAS unsigned char* o = c.L + box;
  const unsigned id = (unsigned)(hh * 16 + kg * 4 + (col & 3));
  *(LAS f32x4*)(o + id * 32) = a0;
  *(LAS f32x4*)(o + id * 32 + 16) = a1;
  if ((lane & 0x33) == 0) *(LAS f32x2*)(o + 2048 + hh * 8) = f32x2{m, l};
  wait_lgkm();
}

__device__ __forceinline__ void stream_role(const Params& p, LAS unsigned char* L, int x, int c28, int w) {
  StreamCtx c;
  const int tid = w * 64 + opaque(lane_id());
  c.rw = mkrs(p.ws, WS_END);
  c.rwv = v4u{(unsigned)(size_t)p.ws, (unsigned)((size_t)p.ws >> 32) & 0xFFFFu, WS_END, 0x00020000u};
  c.L = L; c.lane = tid & 63; c.grp = w >> 2; c.wq = w & 3; c.x = x; c.c28 = c28;
  c.head = 4 * x + c28 / 7;
  c.widx = (c28 % 7) * 4 + c.wq;
  const int a112 = c28 * 4 + c.wq, ka0 = (a112 * 4096) / 112;
  c.nk = ((a112 + 1) * 4096) / 112 - ka0;
  c.ldsA = a_region(c.grp, c28, c.wq);
  c.a_off = (unsigned)((x * SK + ka0) * (DH * 2));
  const int kb0 = (c.widx * 4096) / 28, rem = ((c.widx + 1) * 4096) / 28 - kb0 - 144;
  const unsigned b_off = (unsigned)((c.head * SK + kb0) * (DH * 2));
  constexpr unsigned BATCH_B = NH * SK * DH * 2, OOB = 0x40000000u;
  const rsrc_t rk = mkrs(p.kB, NB * BATCH_B), rv = mkrs(p.vB, NB * BATCH_B);
  const unsigned rec_b = WS_REC + (unsigned)(c.head * HEAD_B);
  constexpr unsigned REC_LAYER = NH * HEAD_B, Q_LAYER = NH * DH * 2;
  const unsigned qa_off = WS_Q + (unsigned)(4 * x * DH * 2), qb_off = WS_Q + (unsigned)(c.head * DH * 2);
  volatile LAS unsigned* fl = flags(c);

  if (tid < 64) ((LAS unsigned*)(L + L_FLG))[tid] = 0u;
  bar();
  v4u K[2 * NS], V[2 * NS];
  SoftState st4 = {-INFINITY, 0.f, {0.f, 0.f, 0.f, 0.f}};
  v4u q4 = {0u, 0u, 0u, 0u};
  unsigned qseq = 1;

#define POFF(i) ((i) == 0 ? 18u * 2048u : (unsigned)((i)-1) * 2048u)
#define RR(i) issue_pair<(i), ((i) < MK_NLO ? MK_THR_LO : THR)>(K, V, rk, rv, ((i) == 0 ? voff0 : voff) | skip, ((i) == 0 ? OOB : voff) | skip, sp + POFF(i))
#define MAIN(i) issue_pair<(NO + (i)) % NS, ((i) >= 12 ? MK_THR_HI : THR)>(K, V, rk, rv, (i) == 0 ? voff0 : voff, (i) == 0 ? OOB : voff, so + POFF(i))
#define MU(i) (2 * ((NO + (i)) % NS))
#define A_PASS1(bb, sq_)                                                            \
  {                                                                                 \
    q_wait<1024>(c, qa_off + (unsigned)((bb) * 4) * Q_LAYER, sq_);                  \
    const AScores sc = a_qk(c);                                                     \
    lds_wait_ge4((volatile LAS v4u*)(fl + 4), (unsigned)(bb));                      \
    a_pv(c, sc, (unsigned)(L_SCR + c.wq * BOX));                                    \
    if (c.lane == 0) fl[8 + c.wq] = (unsigned)((bb) + 1);                           \
    lds_wait_ge4((volatile LAS v4u*)(fl + 8), (unsigned)((bb) + 1));                \
    a_merge<true>(c, bb, 0);                                                        \
    if (c.lane == 0) fl[4 + c.wq] = (unsigned)((bb) + 1);                           \
    SCHED0();                                                                       \
  }
  static_assert(NS == 12 && NO == 7, "static schedule below");

  a_issue(p, c, c.grp);
  const unsigned sq0 = qseq++;
  q_arm<1024>(c, qa_off + (unsigned)(c.grp * 4) * Q_LAYER, sq0);
  if (c.grp == 1) {
    wait_vm<0>();
    A_PASS1(1, sq0)
  }

  for (int it = 0; it < 4; ++it) {
    const int b = 2 * it + c.grp;
    const unsigned so = (unsigned)b * BATCH_B + b_off;
    {
      lds_wait_ge_sleep(fl + c.wq, (unsigned)b);
      const int lane = opaque(c.lane), r = lane & 15;
      const v4u mk = pv_masks(lane);
      const unsigned voff = lane * 16, voff0 = voff | ((lane >> 4) >= rem ? OOB : 0u);
      const bool ok0 = r >= 2 || (r == 0 && (lane >> 4) < rem);
      const unsigned skip = it > 0 ? 0u : OOB, sp = it > 0 ? so - 2 * BATCH_B : so;
      RR(0); RR(1); RR(2); RR(3); RR(4); RR(5); RR(6);
      MAIN(0); MAIN(1);
      if (it > 0) b_chunk<0, 2 * NO, false>(K, V, q4, ok0, st4, r, mk);
      SCHED0();
      MAIN(2); MAIN(3); MAIN(4); MAIN(5); MAIN(6); MAIN(7);
      if (b == 0) A_PASS1(0, sq0)
      MAIN(8); MAIN(9);
      const unsigned sq2 = qseq++;
      if (c.wq == 0 && it > 0) lds_wait_ge4((volatile LAS v4u*)(fl + 32 + 4 * c.grp), (unsigned)it);
      q_arm<256>(c, qb_off + (unsigned)(b * 4 + 1) * Q_LAYER, sq2);
      MAIN(10); MAIN(11);
      q_wait<256>(c, qb_off + (unsigned)(b * 4 + 1) * Q_LAYER, sq2);
      const v4u q2 = *(const LAS v4u*)(L + L_QB + c.grp * 1280 + 1024 + r * 16);
      SoftState st;
      b_chunk<MU(0), 16, true>(K, V, q2, ok0, st, r, mk);
      SCHED0();
      MAIN(12); MAIN(13);
      b_chunk<MU(8), 8, false>(K, V, q2, true, st, r, mk);
      SCHED0();
      MAIN(14); MAIN(15);
      b_chunk<MU(12), 4, false>(K, V, q2, true, st, r, mk);
      SCHED0();
      MAIN(16); MAIN(17);
      b_chunk<MU(14), 4, false>(K, V, q2, true, st, r, mk);
      SCHED0();
      MAIN(18);
      if (lane == 0) fl[c.wq] = (unsigned)(b + 1);
      b_chunk<MU(16), 4, false>(K, V, q2, true, st, r, mk);
      SCHED0();
      b_chunk<MU(18), 2, false>(K, V, q2, true, st, r, mk);
      b_publish(c.rw, rec_b + (unsigned)(b * 4 + 1) * REC_LAYER, c.widx, st, lane);
      if (it > 0) b_publish(c.rw, rec_b + (unsigned)((b - 2) * 4 + 3) * REC_LAYER, c.widx, st4, lane);
    }
    {
      const unsigned sq = qseq++;
      q_arm<1024>(c, qa_off + (unsigned)(b * 4 + 2) * Q_LAYER, sq);
      q_wait<1024>(c, qa_off + (unsigned)(b * 4 + 2) * Q_LAYER, sq);
      const AScores sc = a_qk(c);
      a_pv(c, sc, c.ldsA);
      if (c.lane == 0) fl[16 + 4 * c.grp + c.wq] = (unsigned)(it + 1);
      lds_wait_ge4((volatile LAS v4u*)(fl + 16 + 4 * c.grp), (unsigned)(it + 1));
      a_merge<false>(c, b, 2);
      if (c.lane == 0) fl[24 + 4 * c.grp + c.wq] = (unsigned)(it + 1);
    }
    if (it < 3) {
      lds_wait_ge4((volatile LAS v4u*)(fl + 24 + 4 * c.grp), (unsigned)(it + 1));
      a_issue(p, c, b + 2);
      const unsigned sq = qseq++;
      q_arm<1024>(c, qa_off + (unsigned)((b + 2) * 4) * Q_LAYER, sq);
      wait_vm<0>();
      A_PASS1(b + 2, sq)
    }
    {
      const unsigned sq = qseq++;
      q_arm<256>(c, qb_off + (unsigned)(b * 4 + 3) * Q_LAYER, sq);
      q_wait<256>(c, qb_off + (unsigned)(b * 4 + 3) * Q_LAYER, sq);
      const int lane = opaque(c.lane), r = lane & 15;
      const v4u mk = pv_masks(lane);
      q4 = *(const LAS v4u*)(L + L_QB + c.grp * 1280 + 1024 + r * 16);
      wait_lgkm();
      if (lane == 0) fl[32 + 4 * c.grp + c.wq] = (unsigned)(it + 1);
      b_chunk<0, 2 * NS, true>(K, V, q4, true, st4, r, mk);
      SCHED0();
    }
  }
  {
    const int b = 6 + c.grp;
    const int lane = opaque(c.lane), r = lane & 15;
    const v4u mk = pv_masks(lane);
    const unsigned voff = lane * 16, voff0 = voff | ((lane >> 4) >= rem ? OOB : 0u), skip = 0u;
    const bool ok0 = r >= 2 || (r == 0 && (lane >> 4) < rem);
    const unsigned sp = (unsigned)b * BATCH_B + b_off;
    RR(0); RR(1); RR(2); RR(3); RR(4); RR(5); RR(6);
    b_chunk<0, 2 * NO, false>(K, V, q4, ok0, st4, r, mk);
    b_publish(c.rw, rec_b + (unsigned)(b * 4 + 3) * REC_LAYER, c.widx, st4, lane);
  }
#undef POFF
#undef RR
#undef MAIN
#undef MU
#undef A_PASS1
}

struct WSlice { unsigned w[48]; };

__device__ __forceinline__ void pack_rows(const v4u a, const v4u b, unsigned (&o)[8]) {
  const unsigned av[4] = {a.x, a.y, a.z, a.w}, bv[4] = {b.x, b.y, b.z, b.w};
#pragma unroll
  for (int i = 0; i < 4; ++i) {
    o[2 * i] = __builtin_amdgcn_perm(bv[i], av[i], 0x05040100u);
    o[2 * i + 1] = __builtin_amdgcn_perm(bv[i], av[i], 0x07060302u);
  }
}
__device__ __forceinline__ void pack_slice(const v4u (&raw)[16], WSlice& W, LAS unsigned char* lw) {
#pragma unroll
  for (int n = 0; n < 8; ++n) {
    unsigned o[8];
    pack_rows(raw[2 * n], raw[2 * n + 1], o);
    if (n < 6) {
#pragma unroll
      for (int e = 0; e < 8; ++e) W.w[8 * n + e] = o[e];
    } else {
      *(LAS v4u*)(lw + (2 * (n - 6)) * 8192) = v4u{o[0], o[1], o[2], o[3]};
      *(LAS v4u*)(lw + (2 * (n - 6) + 1) * 8192) = v4u{o[4], o[5], o[6], o[7]};
    }
  }
}
template <bool GQA>
__device__ __forceinline__ void load_wo(const unsigned short* wo, int head, int w, int lane, v4u (&raw)[16]) {
#pragma unroll
  for (int n = 0; n < 8; ++n)
#pragma unroll
    for (int e = 0; e < 2; ++e) {
      const int g = n >> 1, a = n & 1;
      const int k = GQA ? 32 * (w >> 1) + 8 * (2 * e + (g >> 1)) + 4 * (g & 1) + 2 * (w & 1) + a : 32 * (w >> 1) + 8 * g + 4 * (w & 1) + 2 * a + e;
      raw[2 * n + e] = __builtin_nontemporal_load((const v4u*)(wo + (size_t)(head * DH + k) * DM + lane * 8));
    }
}
__device__ __forceinline__ void load_wq(const unsigned short* wq, int head, int w, int lane, v4u (&raw)[16]) {
  const int g = lane >> 4, r = lane & 15;
#pragma unroll
  for (int n = 0; n < 8; ++n)
#pragma unroll
    for (int e = 0; e < 2; ++e) {
      const int k = 64 * w + 4 * (2 * n + e) + g;
      raw[2 * n + e] = __builtin_nontemporal_load((const v4u*)(wq + (size_t)k * (NH * DH) + head * DH + r * 8));
    }
}

struct SvcCtx { rsrc_t rw; LAS unsigned char* L; int head, w, lane; unsigned short* out; unsigned char* ws; };

__device__ __forceinline__ void gemv8(const WSlice& W, const v4u x0, const v4u x1, const v4u x2, const v4u x3, const unsigned (&op)[8], float (&y)[8]) {
#pragma unroll
  for (int e = 0; e < 8; ++e) y[e] = 0.f;
#pragma unroll
  for (int n = 0; n < 6; ++n)
#pragma unroll
    for (int e = 0; e < 8; ++e) y[e] = dot2(W.w[8 * n + e], op[n], y[e]);
#pragma unroll
  for (int e = 0; e < 8; ++e) y[e] = dot2(e < 4 ? x0[e & 3] : x1[e & 3], op[6], y[e]);
#pragma unroll
  for (int e = 0; e < 8; ++e) y[e] = dot2(e < 4 ? x2[e & 3] : x3[e & 3], op[7], y[e]);
}

struct RecRegs { v4u d1, d2; v2u e1, e2; };
__device__ __forceinline__ RecRegs s1_load(const SvcCtx& c, int b, int layer) {
  const int lane = opaque(c.lane), s2 = lane >> 2, pc = lane & 3;
  const unsigned rec = WS_REC + (unsigned)(((b * 4 + layer) * NH + c.head) * HEAD_B);
  const unsigned o1 = s2 * REC_B, o2 = (s2 + 16 < NSRC ? s2 + 16 : s2) * REC_B, po = (4 * c.w + pc) * 16;
  RecRegs r;
  r.e1 = ld_sc1_64(c.rw, o1 + 512, rec); r.e2 = ld_sc1_64(c.rw, o2 + 512, rec);
  r.d1 = ld_sc1(c.rw, o1 + po, rec); r.d2 = ld_sc1(c.rw, o2 + po, rec);
  return r;
}
__device__ __forceinline__ void s1_wait(const SvcCtx& c, int b, int layer, RecRegs& r) {
  for (unsigned spins = 0; spins < SPIN_CAP; ++spins) {
    const unsigned mx = max(max(umax4(r.d1), umax4(r.d2)), max(max(r.e1.x, r.e1.y), max(r.e2.x, r.e2.y)));
    if (all_lanes(mx != SENT)) break;
    CFENCE();
    r = s1_load(c, b, layer);
  }
}
__device__ __forceinline__ void s1_compute(const SvcCtx& c, const WSlice& W, int mat, int b, int layer, const RecRegs& rr, bool pre_bar) {
  const int lane = opaque(c.lane), w = c.w, tid = w * 64 + lane;
  const bool two = (lane >> 2) + 16 < NSRC;
  const LAS unsigned char* lw = c.L + S_W + mat * 32768 + tid * 16;
  const v4u x0 = *(const LAS v4u*)(lw), x1 = *(const LAS v4u*)(lw + 8192), x2 = *(const LAS v4u*)(lw + 16384), x3 = *(const LAS v4u*)(lw + 24576);
  const v4u d1 = rr.d1, d2 = rr.d2;
  const v2u e1 = rr.e1, e2 = rr.e2;
  const float m1 = __uint_as_float(e1.x), m2 = two ? __uint_as_float(e2.x) : -INFINITY;
  float M = fmx(m1, m2);
  M = fmx(M, dppf<ROR4>(M)); M = fmx(M, dppf<XOR8>(M)); M = xrow_max(M);
  const float w1 = ex2((m1 - M) * LOG2E), w2 = ex2((m2 - M) * LOG2E);
  float Ls = w1 * __uint_as_float(e1.y) + w2 * __uint_as_float(e2.y);
  const f32x4 o = __builtin_bit_cast(f32x4, d1) * w1 + __builtin_bit_cast(f32x4, d2) * w2;
  float ov = rs4(o[0], o[1], o[2], o[3]);
  Ls = xrow_sum(Ls);
  ov += dppf<ROR4>(ov); Ls += dppf<ROR4>(Ls);
  ov += dppf<XOR8>(ov); Ls += dppf<XOR8>(Ls);
  ov *= __builtin_amdgcn_rcpf(Ls);
  const unsigned pr = pk2(ov, dppf<XOR1>(ov));
  unsigned op[8];
#pragma unroll
  for (int n = 0; n < 8; ++n) op[n] = __builtin_amdgcn_readlane(pr, 16 * (n >> 1) + 2 * (n & 1));
  float y[8];
  gemv8(W, x0, x1, x2, x3, op, y);
  LAS float* red = (LAS float*)(c.L + S_RED);
  if (pre_bar) bar();
  *(LAS f32x4*)(red + w * DM + lane * 8) = f32x4{y[0], y[1], y[2], y[3]};
  *(LAS f32x4*)(red + w * DM + lane * 8 + 4) = f32x4{y[4], y[5], y[6], y[7]};
  bar();
  {
    const int g = lane >> 4, cq = lane & 15;
    const LAS float* rp = red + 2 * g * DM + 64 * w + 4 * cq;
    const f32x4 s = *(const LAS f32x4*)(rp) + *(const LAS f32x4*)(rp + DM);
    const float v = rs4(s[0], s[1], s[2], s[3]);
    st_sc1_32(c.rw, (unsigned)((64 * w + 4 * cq + g) * 4), WS_Y + (unsigned)(((b * 4 + layer) * NH + c.head) * (DM * 4)), __float_as_uint(v));
  }
}

__device__ __forceinline__ void svc_s1(const SvcCtx& c, const WSlice& W, int mat, int b, int layer) {
  RecRegs r = s1_load(c, b, layer);
  s1_wait(c, b, layer, r);
  s1_compute(c, W, mat, b, layer, r, false);
}

template <bool GATHER>
__device__ __forceinline__ void svc_s2(const SvcCtx& c, const WSlice& W, int mat, int b, int layer) {
  const int lane = opaque(c.lane), w = c.w, g = lane >> 4, cq = lane & 15, tid = w * 64 + lane;
  LAS unsigned short* xs = (LAS unsigned short*)(c.L + S_XS) + b * DM + 64 * w + 4 * cq + g;
  float xv = bf2f(*xs);
  if constexpr (GATHER) {
    const unsigned yo = WS_Y + (unsigned)((b * 4 + layer) * NH * (DM * 4));
    const unsigned vo = (unsigned)(8 * g * (DM * 4) + (64 * w + 4 * cq) * 4);
    v4u d[8];
    unsigned need = 0xFFu;
    for (unsigned spins = 0; need != 0u && spins < SPIN_CAP; ++spins) {
      CFENCE();
#pragma unroll
      for (int i = 0; i < 8; ++i)
        if (need & (1u << i)) d[i] = ld_sc1(c.rw, vo + i * (DM * 4), yo);
      SCHED0();
#pragma unroll
      for (int i = 0; i < 8; ++i)
        if ((need & (1u << i)) && all_lanes(umax4(d[i]) != SENT)) need &= ~(1u << i);
    }
    const f32x4 s = ((__builtin_bit_cast(f32x4, d[0]) + __builtin_bit_cast(f32x4, d[1])) + (__builtin_bit_cast(f32x4, d[2]) + __builtin_bit_cast(f32x4, d[3]))) +
                    ((__builtin_bit_cast(f32x4, d[4]) + __builtin_bit_cast(f32x4, d[5])) + (__builtin_bit_cast(f32x4, d[6]) + __builtin_bit_cast(f32x4, d[7])));
    xv = rbf(xv + rbf(rs4(s[0], s[1], s[2], s[3])));
    *xs = (unsigned short)f2bf(xv);
  }
  CFENCE();
  const LAS unsigned char* lw = c.L + S_W + mat * 32768 + tid * 16;
  const v4u x0 = *(const LAS v4u*)(lw), x1 = *(const LAS v4u*)(lw + 8192), x2 = *(const LAS v4u*)(lw + 16384), x3 = *(const LAS v4u*)(lw + 24576);
  const float piv = __builtin_bit_cast(float, __builtin_amdgcn_readfirstlane(__builtin_bit_cast(int, xv)));
  const float dv = xv - piv;
  const float s1 = xrow_sum(row_sum(dv)), s2 = xrow_sum(row_sum(dv * dv));
  LAS float* stt = (LAS float*)(c.L + S_ST);
  if (lane == 0) *(LAS f32x2*)(stt + 2 * w) = f32x2{piv + s1 * (1.f / 64.f), s2 - s1 * s1 * (1.f / 64.f)};
  bar();
  float mean, M2;
  {
    const f32x2 t = *(const LAS f32x2*)(stt + 2 * (lane & 7));
    float ms = t.x;
    ms += dppf<XOR1>(ms); ms += dppf<XOR2>(ms); ms += dppf<XOR7>(ms);
    mean = ms * 0.125f;
    const float dd = t.x - mean;
    M2 = fmaf(64.f * dd, dd, t.y);
    M2 += dppf<XOR1>(M2); M2 += dppf<XOR2>(M2); M2 += dppf<XOR7>(M2);
  }
  const float mb = rbf(mean), den = rbf(__builtin_amdgcn_sqrtf(rbf(rbf(fmx(M2, 0.f) * (1.f / DM)) + 1e-5f)));
  const float xn = rbf(xv - mb) * __builtin_amdgcn_rcpf(den);
  const unsigned pr = pk2(xn, dppf<XOR1>(xn));
  unsigned xp[8];
  xp[0] = dppu<BCAST + 0>(pr); xp[1] = dppu<BCAST + 2>(pr); xp[2] = dppu<BCAST + 4>(pr); xp[3] = dppu<BCAST + 6>(pr);
  xp[4] = dppu<BCAST + 8>(pr); xp[5] = dppu<BCAST + 10>(pr); xp[6] = dppu<BCAST + 12>(pr); xp[7] = dppu<BCAST + 14>(pr);
  float y[8];
  gemv8(W, x0, x1, x2, x3, xp, y);
  LAS float* red = (LAS float*)(c.L + S_RED2);
  red[w * DH + cq * 8 + g] = rs4(y[0], y[1], y[2], y[3]);
  red[w * DH + cq * 8 + 4 + g] = rs4(y[4], y[5], y[6], y[7]);
  bar();
  if (w == 0) {
    f32x2 t[8];
#pragma unroll
    for (int i = 0; i < 8; ++i) t[i] = *(const LAS f32x2*)(red + i * DH + lane * 2);
    const f32x2 s = ((t[0] + t[1]) + (t[2] + t[3])) + ((t[4] + t[5]) + (t[6] + t[7]));
    st_sc1_32(c.rw, lane * 4, WS_Q + (unsigned)(((b * 4 + layer + 1) * NH + c.head) * (DH * 2)), pk2(s.x, s.y));
  }
}

__device__ __forceinline__ void svc_final(const SvcCtx& c) {
  const int lane = opaque(c.lane), b = c.w, pj = lane >> 2, cq = lane & 3;
  const unsigned yo = WS_Y + (unsigned)((b * 4 + 3) * NH * (DM * 4));
  const unsigned vo = (unsigned)(pj * (DM * 4) + (16 * c.head + 4 * cq) * 4);
  v4u d0, d1;
  for (unsigned spins = 0;; ++spins) {
    CFENCE();
    d0 = ld_sc1(c.rw, vo, yo); d1 = ld_sc1(c.rw, vo + 16 * (DM * 4), yo);
    if (all_lanes(max(umax4(d0), umax4(d1)) != SENT) || spins > SPIN_CAP) break;
  }
  const f32x4 s = __builtin_bit_cast(f32x4, d0) + __builtin_bit_cast(f32x4, d1);
  float v = rs4(s[0], s[1], s[2], s[3]);
  v += dppf<ROR4>(v); v += dppf<XOR8>(v);
  const int colm = b * DM + 16 * c.head + 4 * cq + (lane >> 4);
  const float xo = bf2f(((const LAS unsigned short*)(c.L + S_XS))[colm]);
  if ((lane & 12) == 0) c.out[colm] = (unsigned short)f2bf(xo + rbf(v));
}

__device__ __forceinline__ void service_role(const Params& p, LAS unsigned char* L, int head, int w) {
  SvcCtx c;
  c.lane = opaque(lane_id()); c.w = w;
  const int tid = w * 64 + c.lane;
  c.rw = mkrs(p.ws, WS_END); c.L = L; c.head = head; c.out = p.out; c.ws = p.ws;
  *(LAS v4u*)(L + S_XS + tid * 16) = *(const v4u*)(p.x0 + tid * 8);
  WSlice WqA, WoA, WqB, WoB;
  LAS unsigned char* lw = L + S_W + tid * 16;
  {
    v4u raw[16];
    load_wq(p.wqA, head, c.w, c.lane, raw);
    pack_slice(raw, WqA, lw);
  }
  bar();
  SCHED0();
  {
    v4u raw1[16], raw2[16];
    load_wo<true>(p.woA, head, c.w, c.lane, raw1);
    load_wq(p.wqB, head, c.w, c.lane, raw2);
    SCHED0();
    for (int b = 0; b < 3; ++b) svc_s2<false>(c, WqA, 0, b, -1);
    SCHED0();
    pack_slice(raw1, WoA, lw + 32768);
    pack_slice(raw2, WqB, lw + 2 * 32768);
  }
  svc_s1(c, WoA, 1, 0, 0);
  svc_s2<true>(c, WqB, 2, 0, 0);
  {
    v4u raw[16];
    load_wo<false>(p.woB, head, c.w, c.lane, raw);
    pack_slice(raw, WoB, lw + 3 * 32768);
  }
  for (int b = 0; b <= NB + 1; ++b) {
    if (b < NB) {
      RecRegs r2 = s1_load(c, b, 1);
      s1_wait(c, b, 1, r2);
      const int bn = b + 1 < NB ? b + 1 : b;
      RecRegs r1 = s1_load(c, bn, 0);
      SCHED0();
      s1_compute(c, WoB, 3, b, 1, r2, false);
      if (b + 1 < NB) {
        s1_wait(c, bn, 0, r1);
        s1_compute(c, WoA, 1, bn, 0, r1, true);
      }
      svc_s2<true>(c, WqA, 0, b, 1);
      for (int k = 1; k >= 0; --k) {
        if (k == 0) svc_s1(c, WoA, 1, b, 2);
        if (b + k < NB) svc_s2<true>(c, WqB, 2, b + k, 2 - 2 * k);
      }
    }
    if (b >= 2) {
      svc_s1(c, WoB, 3, b - 2, 3);
      bar();
    }
    if (b + 3 < NB) svc_s2<false>(c, WqA, 0, b + 3, -1);
  }
  svc_final(c);
  bar();
}

__global__ void __launch_bounds__(NTHR, 2) k_mega(Params p) {
  extern __shared__ __attribute__((aligned(16))) unsigned char smem[];
  LAS unsigned char* L = (LAS unsigned char*)smem;
  const int x = blockIdx.x & 7, slot = blockIdx.x >> 3, w = __builtin_amdgcn_readfirstlane(threadIdx.x >> 6);
  if (slot < 4) service_role(p, L, 4 * x + slot, w);
  else stream_role(p, L, x, slot - 4, w);
}
}

extern "C" void kernel_launch(void* const* d_in, const int* in_sizes, int n_in, void* d_out, int out_size, void* d_ws, size_t ws_size, hipStream_t stream) {
  using namespace mk;
  Params p;
  p.x0 = (const unsigned short*)d_in[0];
  p.kA = (const unsigned short*)d_in[1]; p.vA = (const unsigned short*)d_in[2]; p.wqA = (const unsigned short*)d_in[3]; p.woA = (const unsigned short*)d_in[4];
  p.kB = (const unsigned short*)d_in[6]; p.vB = (const unsigned short*)d_in[7]; p.wqB = (const unsigned short*)d_in[8]; p.woB = (const unsigned short*)d_in[9];
  p.out = (unsigned short*)d_out;
  p.ws = (unsigned char*)d_ws;
  (void)hipFuncSetAttribute((const void*)k_mega, hipFuncAttributeMaxDynamicSharedMemorySize, LDS_BYTES);
  (void)hipMemsetAsync(d_ws, 0xFF, WS_END, stream);
  k_mega<<<NWG, NTHR, LDS_BYTES, stream>>>(p);
}
```

```cpp
#include <hip/hip_runtime.h>
#include <hip/hip_bf16.h>
#include <stdint.h>

namespace mk {
typedef unsigned v4u __attribute__((ext_vector_type(4)));
typedef unsigned v2u __attribute__((ext_vector_type(2)));
typedef float f32x4 __attribute__((ext_vector_type(4)));
typedef float f32x2 __attribute__((ext_vector_type(2)));
typedef short s16x4 __attribute__((ext_vector_type(4)));
typedef __bf16 bf16x8 __attribute__((ext_vector_type(8)));
typedef __bf16 bf16x2 __attribute__((ext_vector_type(2)));
typedef __amdgpu_buffer_rsrc_t rsrc_t;
#define LAS __attribute__((address_space(3)))

constexpr int NB = 8, DM = 512, NH = 32, HKV = 8, SK = 4096, DH = 128;
constexpr float SCALE = 0.08838834764831845f;
constexpr float LOG2E = 1.4426950408889634f;
constexpr int NWG = 256, NTHR = 512;
constexpr int NSRC_A = 28, NSRC_B = 7;
constexpr int REC_B = 384;
constexpr unsigned SENT = 0xFFFFFFFFu;
constexpr unsigned SPIN_CAP = 1u << 16;
constexpr unsigned WS_RA = 0, WS_RB = NB * 2 * NH * NSRC_A * REC_B, WS_Y = WS_RB + NB * 2 * NH * NSRC_B * REC_B, WS_Q = WS_Y + NB * 4 * 16 * DM * 4,
                   WS_X = WS_Q + NB * 4 * NH * DH * 2, WS_ARM = WS_X + NB * 4 * DM * 2, WS_FLAG = (WS_ARM + 4095u) / 4096u * 4096u, WS_EPOCH = WS_FLAG + NWG * 64,
                   WS_END = WS_EPOCH + 128;
constexpr int ARM_ITERS = (WS_ARM + NWG * NTHR * 16 - 1) / (NWG * NTHR * 16);
__device__ __forceinline__ unsigned rec_base(int b, int layer, int head) {
  return (layer & 1) ? WS_RB + (unsigned)(((b * 2 + (layer >> 1)) * NH + head) * (NSRC_B * REC_B)) : WS_RA + (unsigned)(((b * 2 + (layer >> 1)) * NH + head) * (NSRC_A * REC_B));
}
constexpr unsigned WS_DBG = 32u << 20;
constexpr int NS = 12;
constexpr int NPMAX = 19, NO = NPMAX - NS;
#define MK_THR 6
#define MK_DELAY0 0
#define MK_PF_W 6
#define MK_POLLMASK 1
#define MK_POLLSKIP 0
#define MK_SPINSLEEP 1
#define MK_THR_LO 6
#define MK_NLO 4
#define MK_THR_HI 8
#define MK_THR_A 20
#define MK_THR_A0 4
constexpr int THR = MK_THR;
constexpr int A_BYTES = 147 * 512, BOX = 2080;
constexpr int L_SCR = 2 * A_BYTES, L_QB = L_SCR + 4 * BOX, L_FLG = L_QB + 2 * 1280, L_BB = L_FLG + 256, L_STAMP = L_BB + 3 * 544, LDS_BYTES = 160 * 1024;
constexpr int S_W = 0, S_RED = 2 * 65536, S_RED2 = S_RED + 8 * DM * 4, S_ST = S_RED2 + 8 * 256 * 4;
static_assert(S_ST + 256 + 2048 <= LDS_BYTES && L_STAMP + 512 <= LDS_BYTES, "LDS map");

struct Params {
  const unsigned short *x0, *kA, *vA, *wqA, *woA, *kB, *vB, *wqB, *woB;
  unsigned short* out;
  unsigned char* ws;
};

__device__ __forceinline__ float bf2f(unsigned b) { return __builtin_bit_cast(float, b << 16); }
__device__ __forceinline__ unsigned f2bf(float f) { return (unsigned)__builtin_bit_cast(unsigned short, (__bf16)f); }
__device__ __forceinline__ float rbf(float f) { return bf2f(f2bf(f)); }
__device__ __forceinline__ unsigned pk2(float lo, float hi) { return __builtin_bit_cast(unsigned, bf16x2{(__bf16)lo, (__bf16)hi}); }
__device__ __forceinline__ float dot2(unsigned a, unsigned b, float c) {
  return __builtin_amdgcn_fdot2_f32_bf16(__builtin_bit_cast(bf16x2, a), __builtin_bit_cast(bf16x2, b), c, false);
}
template <int CTRL> __device__ __forceinline__ float dppf(float x) {
  return __builtin_bit_cast(float, __builtin_amdgcn_mov_dpp(__builtin_bit_cast(int, x), CTRL, 0xf, 0xf, true));
}
template <int CTRL> __device__ __forceinline__ unsigned dppu(unsigned x) { return (unsigned)__builtin_amdgcn_mov_dpp((int)x, CTRL, 0xf, 0xf, true); }
constexpr int XOR1 = 0xB1, XOR2 = 0x4E, XOR7 = 0x141, XOR8 = 0x128, ROR4 = 0x124, BCAST = 0x150;
__device__ __forceinline__ float fmx(float a, float b) { return __builtin_fmaxf(a, b); }
__device__ __forceinline__ float xrow_max(float x) {
  auto s = __builtin_amdgcn_permlane16_swap(__float_as_uint(x), __float_as_uint(x), false, false);
  x = fmx(__uint_as_float(s[0]), __uint_as_float(s[1]));
  auto t = __builtin_amdgcn_permlane32_swap(__float_as_uint(x), __float_as_uint(x), false, false);
  return fmx(__uint_as_float(t[0]), __uint_as_float(t[1]));
}
__device__ __forceinline__ float xrow_sum(float x) {
  auto s = __builtin_amdgcn_permlane16_swap(__float_as_uint(x), __float_as_uint(x), false, false);
  x = __uint_as_float(s[0]) + __uint_as_float(s[1]);
  auto t = __builtin_amdgcn_permlane32_swap(__float_as_uint(x), __float_as_uint(x), false, false);
  return __uint_as_float(t[0]) + __uint_as_float(t[1]);
}
__device__ __forceinline__ float rs4(float a, float b, float c, float d) {
  auto s = __builtin_amdgcn_permlane16_swap(__float_as_uint(a), __float_as_uint(b), false, false);
  auto t = __builtin_amdgcn_permlane16_swap(__float_as_uint(c), __float_as_uint(d), false, false);
  const float u = __uint_as_float(s[0]) + __uint_as_float(s[1]), v = __uint_as_float(t[0]) + __uint_as_float(t[1]);
  auto r = __builtin_amdgcn_permlane32_swap(__float_as_uint(u), __float_as_uint(v), false, false);
  return __uint_as_float(r[0]) + __uint_as_float(r[1]);
}
__device__ __forceinline__ float row_max(float x) {
  x = fmx(x, dppf<XOR1>(x)); x = fmx(x, dppf<XOR2>(x)); x = fmx(x, dppf<ROR4>(x)); return fmx(x, dppf<XOR8>(x));
}
__device__ __forceinline__ float row_sum(float x) {
  x += dppf<XOR1>(x); x += dppf<XOR2>(x); x += dppf<ROR4>(x); return x + dppf<XOR8>(x);
}
__device__ __forceinline__ float ex2(float x) { return __builtin_amdgcn_exp2f(x); }
__device__ __forceinline__ rsrc_t mkrs(const void* p, unsigned bytes) { return __builtin_amdgcn_make_buffer_rsrc((void*)p, 0, (int)bytes, 0x00020000); }
__device__ __forceinline__ v4u ld_nt(rsrc_t rs, unsigned voff, unsigned soff) { return __builtin_amdgcn_raw_buffer_load_b128(rs, voff, soff, 2); }
__device__ __forceinline__ v4u ld_sc1(rsrc_t rs, unsigned voff, unsigned soff) { return __builtin_amdgcn_raw_buffer_load_b128(rs, voff, soff, 16); }
__device__ __forceinline__ v2u ld_sc1_64(rsrc_t rs, unsigned voff, unsigned soff) { return __builtin_amdgcn_raw_buffer_load_b64(rs, voff, soff, 16); }
__device__ __forceinline__ unsigned nosent(unsigned x) { return x < 0xFFFFFFFEu ? x : 0xFFFFFFFEu; }
__device__ __forceinline__ void st_sc1(rsrc_t rs, unsigned voff, unsigned soff, v4u v) {
  v.x = nosent(v.x); v.y = nosent(v.y); v.z = nosent(v.z); v.w = nosent(v.w);
  __builtin_amdgcn_raw_buffer_store_b128(v, rs, voff + soff, 0, 16);
}
__device__ __forceinline__ void st_sc1_64(rsrc_t rs, unsigned voff, unsigned soff, v2u v) {
  v.x = nosent(v.x); v.y = nosent(v.y);
  __builtin_amdgcn_raw_buffer_store_b64(v, rs, voff, soff, 16);
}
__device__ __forceinline__ void st_sc1_32(rsrc_t rs, unsigned voff, unsigned soff, unsigned v) { __builtin_amdgcn_raw_buffer_store_b32(nosent(v), rs, voff, soff, 16); }
__device__ __forceinline__ unsigned umax4(v4u v) { return max(max(v.x, v.y), max(v.z, v.w)); }
__device__ __forceinline__ bool all_lanes(bool ok) { return __builtin_amdgcn_ballot_w64(!ok) == 0ull; }
#define CFENCE() asm volatile("" ::: "memory")
__device__ __forceinline__ int lane_id() { return (int)__builtin_amdgcn_mbcnt_hi(~0u, __builtin_amdgcn_mbcnt_lo(~0u, 0u)); }
__device__ __forceinline__ int opaque(int v) { asm volatile("" : "+v"(v)); return v; }
#define SCHED0() __builtin_amdgcn_sched_barrier(0)
template <int N> __device__ __forceinline__ void wait_vm() { asm volatile("s_waitcnt vmcnt(%0)" ::"n"(N) : "memory"); }
__device__ __forceinline__ void bar() { asm volatile("s_waitcnt lgkmcnt(0)\n\ts_barrier" ::: "memory"); }
__device__ __forceinline__ void wait_lgkm() { asm volatile("s_waitcnt lgkmcnt(0)" ::: "memory"); }
__device__ __forceinline__ f32x4 mfma16(unsigned a0, unsigned a1, unsigned b0, unsigned b1, f32x4 c) {
  return __builtin_amdgcn_mfma_f32_16x16x16bf16_1k(__builtin_bit_cast(s16x4, v2u{a0, a1}), __builtin_bit_cast(s16x4, v2u{b0, b1}), c, 0, 0, 0);
}
__device__ __forceinline__ f32x4 mfma32(v4u a, v4u b, f32x4 c) {
  return __builtin_amdgcn_mfma_f32_16x16x32_bf16(__builtin_bit_cast(bf16x8, a), __builtin_bit_cast(bf16x8, b), c, 0, 0, 0);
}
typedef _Float16 h16x2 __attribute__((ext_vector_type(2)));
__device__ __forceinline__ unsigned pkh(float lo, float hi) { return __builtin_bit_cast(unsigned, h16x2{(_Float16)lo, (_Float16)hi}); }
__device__ __forceinline__ f32x2 rec_scale(float amax) {
  unsigned e = __float_as_uint(amax) & 0x7F800000u;
  e = min(max(e, 0x01000000u), 0x7E000000u);
  return f32x2{__uint_as_float(e), __uint_as_float(0x7F000000u - e)};
}
__device__ __forceinline__ unsigned arm_buffers(rsrc_t rw, int tid) {
  const unsigned e = __builtin_amdgcn_raw_buffer_load_b32(rw, 0, WS_EPOCH, 16);
  const unsigned sv = (unsigned)opaque(-1);
#pragma unroll
  for (int i = 0; i < ARM_ITERS; ++i) {
    const unsigned off = ((blockIdx.x * ARM_ITERS + i) * NTHR + tid) * 16u;
    if (off < WS_ARM) __builtin_amdgcn_raw_buffer_store_b128(v4u{sv, sv, sv, sv}, rw, off, 0, 16);
  }
  const unsigned tag = __builtin_amdgcn_readfirstlane(e) * 2654435761u + 12345u;
  wait_vm<0>();
  bar();
  if (tid == 0) __builtin_amdgcn_raw_buffer_store_b32(tag, rw, blockIdx.x * 64u, WS_FLAG, 16);
  return tag;
}
__device__ __forceinline__ void armed_wait(rsrc_t rw, unsigned tag, int lane) {
  for (unsigned spins = 0; spins < SPIN_CAP; ++spins) {
    CFENCE();
    bool ok = true;
#pragma unroll
    for (int i = 0; i < 4; ++i) ok &= __builtin_amdgcn_raw_buffer_load_b32(rw, (unsigned)(i * 64 + lane) * 64u, WS_FLAG, 16) == tag;
    if (all_lanes(ok)) break;
    __builtin_amdgcn_s_sleep(4);
  }
  CFENCE();
}
__device__ __forceinline__ void lds_wait_ge(volatile LAS unsigned* f, unsigned v) {
  for (unsigned spins = 0; *f < v && spins < (SPIN_CAP << 7); ++spins) __builtin_amdgcn_s_sleep(1);
  CFENCE();
}
__device__ __forceinline__ void lds_wait_ge_sleep(volatile LAS unsigned* f, unsigned v) {
  for (unsigned spins = 0; *f < v && spins < (SPIN_CAP << 6); ++spins) __builtin_amdgcn_s_sleep(2);
  CFENCE();
}
__device__ __forceinline__ void lds_wait_ge4(volatile LAS v4u* f, unsigned v) {
  for (unsigned spins = 0; spins < (SPIN_CAP << 8); ++spins) {
    const v4u t = *f;
    if (min(min(t.x, t.y), min(t.z, t.w)) >= v) break;
  }
  CFENCE();
}

struct SoftState { float m, l; f32x4 a; };

template <int U0, int NU>
__device__ __forceinline__ float b_scores(const v4u (&K)[2 * NS], const v4u q, int r) {
  constexpr int P2 = NU > 8 ? 16 : NU > 4 ? 8 : NU > 2 ? 4 : 2;
  float t[16];
#pragma unroll
  for (int u = 0; u < NU; ++u) {
    const v4u k = K[(U0 + u) % (2 * NS)];
    t[u] = dot2(k.w, q.w, dot2(k.z, q.z, dot2(k.y, q.y, dot2(k.x, q.x, 0.f))));
  }
#define TR_STEP(HALF, CTRL, CNT)                                                                  \
  _Pragma("unroll") for (int i = 0; i < (HALF); ++i) {                                            \
    if (i < (CNT)) {                                                                              \
      const float a_ = t[i] + dppf<CTRL>(t[i]);                                                   \
      if (i + (HALF) < (CNT)) { const float b_ = t[i + (HALF)] + dppf<CTRL>(t[i + (HALF)]); t[i] = (r & (HALF)) ? b_ : a_; } \
      else t[i] = a_;                                                                             \
    }                                                                                             \
  }
  if constexpr (P2 >= 16) { TR_STEP(8, XOR8, NU) }
  if constexpr (P2 >= 8) { TR_STEP(4, XOR7, (NU < 8 ? NU : 8)) }
  if constexpr (P2 >= 4) { TR_STEP(2, XOR2, (NU < 4 ? NU : 4)) }
  TR_STEP(1, XOR1, 2)
#undef TR_STEP
  float x = t[0];
  if constexpr (P2 < 4) x += dppf<XOR2>(x);
  if constexpr (P2 < 8) x += dppf<ROR4>(x);
  if constexpr (P2 < 16) x += dppf<XOR8>(x);
  return x;
}
template <int U0, int NU>
__device__ __forceinline__ f32x4 b_pv(const v4u (&V)[2 * NS], unsigned pp, const v4u mk, f32x4 a) {
#define PV(U)                                                                                     \
  if constexpr ((U) < NU) {                                                                       \
    const unsigned bc = dppu<BCAST + (U)>(pp);                                                    \
    a = mfma32(V[(U0 + (U)) % (2 * NS)], v4u{bc & mk.x, bc & mk.y, bc & mk.z, bc & mk.w}, a);     \
  }
  PV(0) PV(1) PV(2) PV(3) PV(4) PV(5) PV(6) PV(7) PV(8) PV(9) PV(10) PV(11) PV(12) PV(13) PV(14) PV(15)
#undef PV
  return a;
}
template <int U0, int NU, bool FIRST>
__device__ __forceinline__ void b_chunk(const v4u (&K)[2 * NS], const v4u (&V)[2 * NS], const v4u q, bool ok0, SoftState& st, int r, const v4u mk) {
  constexpr int N0 = NU > 16 ? 16 : NU, N1 = NU - N0;
  const float x0 = b_scores<U0, N0>(K, q, r);
  const float s0 = (r < N0 && ok0) ? rbf(rbf(x0) * SCALE) : -INFINITY;
  float s1 = -INFINITY;
  if constexpr (N1 > 0) {
    const float x1 = b_scores<U0 + 16, N1>(K, q, r);
    s1 = r < N1 ? rbf(rbf(x1) * SCALE) : -INFINITY;
  }
  float mn = xrow_max(row_max(fmx(s0, s1)));
  if constexpr (!FIRST) mn = fmx(mn, st.m);
  const float p0 = ex2((s0 - mn) * LOG2E);
  if constexpr (FIRST) {
    st.l = p0;
    st.a = f32x4{0.f, 0.f, 0.f, 0.f};
  } else {
    const float alpha = ex2((st.m - mn) * LOG2E);
    st.l = st.l * alpha + p0;
    st.a *= alpha;
  }
  st.m = mn;
  st.a = b_pv<U0, N0>(V, pk2(p0, p0), mk, st.a);
  if constexpr (N1 > 0) {
    const float p1 = ex2((s1 - mn) * LOG2E);
    st.l += p1;
    st.a = b_pv<U0 + 16, N1>(V, pk2(p1, p1), mk, st.a);
  }
}
__device__ __forceinline__ v4u pv_masks(int lane) {
  const unsigned h = (lane & 1) ? 0xFFFF0000u : 0xFFFFu;
  const int t = (lane >> 1) & 3;
  return v4u{t == 0 ? h : 0u, t == 1 ? h : 0u, t == 2 ? h : 0u, t == 3 ? h : 0u};
}

template <int SLOT, int TH>
__device__ __forceinline__ void issue_pair(v4u (&K)[2 * NS], v4u (&V)[2 * NS], rsrc_t rk, rsrc_t rv, unsigned va, unsigned vb, unsigned soff) {
  wait_vm<TH>();
  K[2 * SLOT] = ld_nt(rk, va, soff);
  K[2 * SLOT + 1] = ld_nt(rk, vb + 1024, soff);
  SCHED0();
  wait_vm<TH>();
  V[2 * SLOT] = ld_nt(rv, va, soff);
  V[2 * SLOT + 1] = ld_nt(rv, vb + 1024, soff);
  SCHED0();
}

struct StreamCtx {
  rsrc_t rw;
  v4u rwv;
  LAS unsigned char* L;
  int lane, grp, wq, x, head, c28, widx;
  int nk;
  unsigned ldsA;
  unsigned a_off, a_tail;
};
__device__ __forceinline__ volatile LAS unsigned* flags(const StreamCtx& c) { return (volatile LAS unsigned*)(c.L + L_FLG); }
__device__ __forceinline__ unsigned a_region(int grp, int c28, int wq) {
  return (unsigned)(grp * A_BYTES + (((c28 * 4 + wq) * 4096) / 112 - (c28 * 16384) / 112) * 512);
}

template <int TH>
__device__ __forceinline__ void a_issue(const Params& p, const StreamCtx& c, int b, unsigned region) {
  const int lane = opaque(c.lane), a = lane >> 4, pos = lane & 15;
  const rsrc_t rk = mkrs(p.kA, NB * HKV * SK * DH * 2), rv = mkrs(p.vA, NB * HKV * SK * DH * 2);
  const unsigned so = (unsigned)b * (HKV * SK * DH * 2) + c.a_off;
  const unsigned kv = a * 256 + ((pos ^ (4 * a)) * 16), vv = lane * 16;
  LAS unsigned char* kd = c.L + region;
  LAS unsigned char* vd = kd + c.nk * 256;
  if (c.nk == 37 && lane < 16) {
    __builtin_amdgcn_raw_ptr_buffer_load_lds(rk, (LAS void*)(kd + 9 * 1024), 16, kv ^ 16, so + c.a_tail, 0, 2);
    __builtin_amdgcn_raw_ptr_buffer_load_lds(rv, (LAS void*)(vd + 9 * 1024), 16, vv, so + c.a_tail, 0, 2);
  }
#pragma unroll
  for (int u = 0; u < 9; ++u) {
    wait_vm<TH>();
    __builtin_amdgcn_raw_ptr_buffer_load_lds(rk, (LAS void*)(kd + u * 1024), 16, kv ^ ((u & 3) * 16), so + u * 4096, 0, 2);
    __builtin_amdgcn_raw_ptr_buffer_load_lds(rv, (LAS void*)(vd + u * 1024), 16, vv, so + u * 4096, 0, 2);
  }
}

__device__ __forceinline__ void b_publish(const StreamCtx& c, int b, int layer, const SoftState& st, unsigned seq) {
  const int lane = opaque(c.lane), cc = lane & 15, lg = lane >> 4;
  volatile LAS unsigned* fl = flags(c);
  float l = xrow_sum(row_sum(st.l)), m = st.m;
  const unsigned pos = (unsigned)(lg * 8 + (cc & 7)) * 16u;
  if (c.wq < 3) {
    LAS unsigned char* o = c.L + L_BB + c.wq * 544;
    lds_wait_ge(fl + 47, seq - 1);
    if (cc < 8) *(LAS f32x4*)(o + pos) = st.a;
    if (lane == 0) *(LAS f32x2*)(o + 512) = f32x2{m, l};
    wait_lgkm();
    if (lane == 0) fl[44 + c.wq] = seq;
  } else {
    for (unsigned spins = 0; spins < (SPIN_CAP << 8); ++spins) {
      const v4u t = *(volatile LAS v4u*)(fl + 44);
      if (min(min(t.x, t.y), t.z) >= seq) break;
    }
    CFENCE();
    f32x4 a[3]; f32x2 ml[3];
#pragma unroll
    for (int i = 0; i < 3; ++i) { a[i] = *(const LAS f32x4*)(c.L + L_BB + i * 544 + pos); ml[i] = *(const LAS f32x2*)(c.L + L_BB + i * 544 + 512); }
    const float M = fmx(fmx(m, ml[0].x), fmx(ml[1].x, ml[2].x));
    const float e = ex2((m - M) * LOG2E);
    f32x4 o = st.a * e;
    l *= e;
#pragma unroll
    for (int i = 0; i < 3; ++i) { const float ei = ex2((ml[i].x - M) * LOG2E); o += a[i] * ei; l = fmaf(ml[i].y, ei, l); }
    CFENCE();
    if (lane == 0) fl[47] = seq;
    const f32x2 sc = rec_scale(xrow_max(row_max(fmx(fmx(fabsf(o[0]), fabsf(o[1])), fmx(fabsf(o[2]), fabsf(o[3]))))));
    o *= sc.y;
    const unsigned ro = rec_base(b, layer, c.head) + (unsigned)((c.c28 % 7) * REC_B);
    if (cc < 8) st_sc1_64(c.rw, pos >> 1, ro, v2u{pkh(o[0], o[1]), pkh(o[2], o[3])});
    if (lane == 8) st_sc1(c.rw, 256, ro, v4u{__float_as_uint(M), __float_as_uint(l), __float_as_uint(sc.x), 0u});
  }
  CFENCE();
}

template <bool SHARED>
__device__ __forceinline__ void a_merge(const StreamCtx& c, int b, int layer) {
  const int lane = opaque(c.lane), kg = lane >> 4, col = lane & 15, wq = c.wq;
  unsigned src;
  if constexpr (SHARED) src = (unsigned)(L_SCR + kg * BOX);
  else {
    const unsigned r0 = a_region(c.grp, c.c28, 0), r1 = a_region(c.grp, c.c28, 1), r2 = a_region(c.grp, c.c28, 2), r3 = a_region(c.grp, c.c28, 3);
    src = (kg & 2) ? ((kg & 1) ? r3 : r2) : ((kg & 1) ? r1 : r0);
  }
  const LAS unsigned char* in = c.L + src;
  const f32x2 ml = *(const LAS f32x2*)(in + 2048 + wq * 8);
  f32x4 b0 = *(const LAS f32x4*)(in + (wq * 16 + col) * 32), b1 = *(const LAS f32x4*)(in + (wq * 16 + col) * 32 + 16);
  const float M = xrow_max(ml.x), e = ex2((ml.x - M) * LOG2E);
  b0 *= e; b1 *= e;
  const float Lt = xrow_sum(ml.y * e);
  const float o0 = rs4(b0[0], b0[1], b0[2], b0[3]), o1 = rs4(b1[0], b1[1], b1[2], b1[3]);
  const f32x2 sc = rec_scale(xrow_max(row_max(fmx(fabsf(o0), fabsf(o1)))));
  const unsigned ro = rec_base(b, layer, 4 * c.x + wq) + (unsigned)(c.c28 * REC_B);
  st_sc1_32(c.rw, (unsigned)(col * 16 + kg * 4), ro, pkh(o0 * sc.y, o1 * sc.y));
  if (lane == 0) st_sc1(c.rw, 256, ro, v4u{__float_as_uint(M), __float_as_uint(Lt), __float_as_uint(sc.x), 0u});
  CFENCE();
}
template <int NBYTES>
__device__ __forceinline__ void q_dma(const StreamCtx& c, unsigned qoff) {
  const unsigned dst = (unsigned)(size_t)(c.L + L_QB + c.grp * 1280 + (NBYTES == 1024 ? 0 : 1024));
  const unsigned voff = opaque(c.lane) * 16;
  if (voff < NBYTES) {
    unsigned keep;
    asm volatile("s_mov_b32 %0, m0\n\ts_mov_b32 m0, %1\n\ts_nop 0\n\tbuffer_load_dwordx4 %2, %3, %4 offen sc1 lds\n\ts_mov_b32 m0, %0"
                 : "=&s"(keep) : "s"(dst), "v"(voff), "s"(c.rwv), "s"(qoff) : "memory");
  }
}
template <int NBYTES>
__device__ __forceinline__ void q_arm(const StreamCtx& c, unsigned qoff, unsigned seq) {
  if (c.wq == 0) {
    const int lane = opaque(c.lane);
    const unsigned sv = (unsigned)opaque(-1);
    *(LAS v4u*)(c.L + L_QB + c.grp * 1280 + (NBYTES == 1024 ? 0 : 1024) + ((lane * 16) & (NBYTES - 1))) = v4u{sv, sv, sv, sv};
    wait_lgkm();
    if (lane == 0) flags(c)[12 + c.grp] = seq;
    q_dma<NBYTES>(c, qoff);
  }
}
template <int NBYTES>
__device__ __forceinline__ void q_wait(const StreamCtx& c, unsigned qoff, unsigned seq) {
  const int lane = opaque(c.lane);
  volatile LAS v4u* qp = (volatile LAS v4u*)(c.L + L_QB + c.grp * 1280 + (NBYTES == 1024 ? 0 : 1024) + ((lane * 16) & (NBYTES - 1)));
  volatile LAS unsigned* armed = flags(c) + 12 + c.grp;
  for (unsigned spins = 0; spins < (SPIN_CAP << 6); ++spins) {
    const unsigned a = *armed;
    const v4u q = *qp;
    if (all_lanes(a >= seq && umax4(q) != SENT)) break;
    if (c.wq == 0 && (spins & MK_POLLMASK) == MK_POLLMASK && spins >= MK_POLLSKIP) q_dma<NBYTES>(c, qoff);
    __builtin_amdgcn_s_sleep(MK_SPINSLEEP);
  }
  CFENCE();
}

struct AScores { float s[10]; };
__device__ __forceinline__ AScores a_qk(const StreamCtx& c, unsigned region) {
  const int lane = opaque(c.lane), kg = lane >> 4, col = lane & 15, hh = col >> 2;
  const LAS unsigned char* base = c.L + region;
  const LAS unsigned char* qb = c.L + L_QB + c.grp * 1280 + hh * 256 + kg * 16;
  f32x4 sc[3] = {{0.f, 0.f, 0.f, 0.f}, {0.f, 0.f, 0.f, 0.f}, {0.f, 0.f, 0.f, 0.f}};
  const unsigned ro = (unsigned)((col & 3) * 1024 + (col >> 2) * 256);
#pragma unroll
  for (int th = 0; th < 2; ++th) {
    v4u qf[2], kf[2][3];
#pragma unroll
    for (int t2 = 0; t2 < 2; ++t2) {
      const int t = 2 * th + t2;
      const LAS unsigned char* kp = base + ro + 16 * ((4 * t + kg) ^ col);
      qf[t2] = *(const LAS v4u*)(qb + t * 64);
#pragma unroll
      for (int tau = 0; tau < 3; ++tau) kf[t2][tau] = *(const LAS v4u*)(kp + tau * 4096);
    }
    SCHED0();
#pragma unroll
    for (int t2 = 0; t2 < 2; ++t2)
#pragma unroll
      for (int tau = 0; tau < 3; ++tau) sc[tau] = mfma32(kf[t2][tau], qf[t2], sc[tau]);
  }
  AScores o;
#pragma unroll
  for (int u = 0; u < 10; ++u) o.s[u] = rbf(rbf(sc[u >> 2][u & 3]) * SCALE);
  if (!(c.nk == 37 && kg == 0)) o.s[9] = -INFINITY;
  return o;
}
__device__ __forceinline__ void a_pv(const StreamCtx& c, const AScores& sc, unsigned region, unsigned box) {
  const int lane = opaque(c.lane), kg = lane >> 4, col = lane & 15, hh = col >> 2;
  const unsigned m0 = (lane & 3) == 0 ? 0xFFFFu : (lane & 3) == 1 ? 0xFFFF0000u : 0u;
  const unsigned m1 = (lane & 3) == 2 ? 0xFFFFu : (lane & 3) == 3 ? 0xFFFF0000u : 0u;
  const LAS unsigned char* vbase = c.L + region + c.nk * 256 + lane * 16;
  float mx = sc.s[0];
#pragma unroll
  for (int u = 1; u < 10; ++u) mx = fmx(mx, sc.s[u]);
  const float m = xrow_max(mx), mneg = -m * LOG2E;
  float l = 0.f;
  f32x4 a0 = {0.f, 0.f, 0.f, 0.f}, a1 = {0.f, 0.f, 0.f, 0.f};
#pragma unroll
  for (int u = 0; u < 10; ++u) {
    if (u < 9 || c.nk == 37) {
      const float pr = ex2(fmaf(sc.s[u], LOG2E, mneg));
      l += pr;
      const unsigned pp = pk2(pr, pr), b0 = pp & m0, b1 = pp & m1;
      v4u v = *(const LAS v4u*)(vbase + u * 1024);
      if (u == 9 && kg != 0) v = v4u{0u, 0u, 0u, 0u};
      a0 = mfma16(v.x, v.y, b0, b1, a0);
      a1 = mfma16(v.z, v.w, b0, b1, a1);
    }
  }
  l = xrow_sum(l);
  LAS unsigned char* o = c.L + box;
  const unsigned id = (unsigned)(hh * 16 + kg * 4 + (col & 3));
  *(LAS f32x4*)(o + id * 32) = a0;
  *(LAS f32x4*)(o + id * 32 + 16) = a1;
  if ((lane & 0x33) == 0) *(LAS f32x2*)(o + 2048 + hh * 8) = f32x2{m, l};
  wait_lgkm();
}

__device__ __forceinline__ void stream_role(const Params& p, LAS unsigned char* L, int x, int c28, int w) {
  StreamCtx c;
  const int tid = w * 64 + opaque(lane_id());
  c.rw = mkrs(p.ws, WS_END);
  c.rwv = v4u{(unsigned)(size_t)p.ws, (unsigned)((size_t)p.ws >> 32) & 0xFFFFu, WS_END, 0x00020000u};
  c.L = L; c.lane = tid & 63; c.grp = w >> 2; c.wq = w & 3; c.x = x; c.c28 = c28;
  c.head = 4 * x + c28 / 7;
  c.widx = (c28 % 7) * 4 + c.wq;
  const int a112 = c28 * 4 + c.wq, ka0 = (a112 * 4096) / 112;
  c.nk = ((a112 + 1) * 4096) / 112 - ka0;
  c.ldsA = a_region(c.grp, c28, c.wq);
  const int ka_cu = (c28 * 16384) / 112;
  c.a_off = (unsigned)((x * SK + ka_cu) * (DH * 2)) + (unsigned)c.wq * 1024u;
  c.a_tail = (unsigned)((144 + (ka0 - ka_cu) - 36 * c.wq) * (DH * 2)) - (unsigned)c.wq * 1024u;
  const int c7 = c28 % 7, kb0 = (c7 * 4096) / 7, ntail = ((c7 + 1) * 4096) / 7 - kb0 - 576;
  const int t0 = (c.wq * ntail) / 4, rem = ((c.wq + 1) * ntail) / 4 - t0;
  const unsigned b_off = (unsigned)((c.head * SK + kb0) * (DH * 2)) + (unsigned)c.wq * 2048u;
  const unsigned p0_off = (unsigned)((576 + t0) * (DH * 2)) - (unsigned)c.wq * 2048u;
  constexpr unsigned BATCH_B = NH * SK * DH * 2, OOB = 0x40000000u;
  const rsrc_t rk = mkrs(p.kB, NB * BATCH_B), rv = mkrs(p.vB, NB * BATCH_B);
  constexpr unsigned Q_LAYER = NH * DH * 2;
  const unsigned qa_off = WS_Q + (unsigned)(4 * x * DH * 2), qb_off = WS_Q + (unsigned)(c.head * DH * 2);
  volatile LAS unsigned* fl = flags(c);
  if (tid < 64) ((LAS unsigned*)(L + L_FLG))[tid] = 0u;
  const unsigned tag = arm_buffers(mkrs(p.ws, WS_END), tid);

  v4u K[2 * NS], V[2 * NS];
  SoftState st4 = {-INFINITY, 0.f, {0.f, 0.f, 0.f, 0.f}};
  v4u q4 = {0u, 0u, 0u, 0u};
  unsigned qseq = 1;

#define POFF(i) ((i) == 0 ? p0_off : (unsigned)((i)-1) * 8192u)
#define RR(i) issue_pair<(i), ((i) < MK_NLO ? MK_THR_LO : THR)>(K, V, rk, rv, ((i) == 0 ? voff0 : voff) | skip, ((i) == 0 ? OOB : voff) | skip, sp + POFF(i))
#define MAIN(i) issue_pair<(NO + (i)) % NS, ((i) >= 12 ? MK_THR_HI : THR)>(K, V, rk, rv, (i) == 0 ? voff0 : voff, (i) == 0 ? OOB : voff, so + POFF(i))
#define MU(i) (2 * ((NO + (i)) % NS))
#define A_PASS1(bb, sq_, reg_)                                                        \
  {                                                                                 \
    q_wait<1024>(c, qa_off + (unsigned)((bb) * 4) * Q_LAYER, sq_);                  \
    const AScores sc = a_qk(c, reg_);                                               \
    lds_wait_ge4((volatile LAS v4u*)(fl + 4), (unsigned)(bb));                      \
    a_pv(c, sc, reg_, (unsigned)(L_SCR + c.wq * BOX));                              \
    if (c.lane == 0) fl[8 + c.wq] = (unsigned)((bb) + 1);                           \
    lds_wait_ge4((volatile LAS v4u*)(fl + 8), (unsigned)((bb) + 1));                \
    a_merge<true>(c, bb, 0);                                                        \
    if (c.lane == 0) fl[4 + c.wq] = (unsigned)((bb) + 1);                           \
    SCHED0();                                                                       \
  }
  static_assert(NS == 12 && NO == 7, "static schedule below");

  if (c.grp == 1) {
#pragma unroll 1
    for (int bb = 0; bb < 2; ++bb) {
      a_issue<MK_THR_A0>(p, c, bb, c.ldsA - (1 - bb) * A_BYTES);
      wait_vm<0>();
      if (bb == 0 && c.wq == 0) armed_wait(c.rw, tag, opaque(c.lane));
      const unsigned sq = qseq++;
      q_arm<1024>(c, qa_off + (unsigned)(bb * 4) * Q_LAYER, sq);
      A_PASS1(bb, sq, c.ldsA - (1 - bb) * A_BYTES)
    }
  } else {
    const int lane = opaque(c.lane);
#pragma unroll
    for (int i = 0; i < 19; ++i) {
      const int ch = min(i * 112 + a112, 2047), row = (ch >> 1) & 511;
      const unsigned short* src = (ch & 1) ? ((ch >> 10) ? p.woB : p.woA) + (size_t)(512 * x + row) * DM
                                           : ((ch >> 10) ? p.wqB : p.wqA) + (size_t)row * (NH * DH) + 512 * x;
      if (i >= MK_PF_W) wait_vm<MK_PF_W>();
      K[i] = __builtin_nontemporal_load((const v4u*)(src + lane * 8));
    }
#pragma unroll
    for (int i = 0; i < 19; ++i) asm volatile("" ::"v"(K[i]));
    if (c.wq == 0) armed_wait(c.rw, tag, lane);
  }

  for (int it = 0; it < 4; ++it) {
    const int b = 2 * it + c.grp;
    const unsigned so = (unsigned)b * BATCH_B + b_off;
    {
      lds_wait_ge_sleep(fl + c.wq, (unsigned)b);
      const int lane = opaque(c.lane), r = lane & 15;
      const v4u mk = pv_masks(lane);
      const unsigned voff = lane * 16, voff0 = voff | ((lane >> 4) >= rem ? OOB : 0u);
      const bool ok0 = r >= 2 || (r == 0 && (lane >> 4) < rem);
      const unsigned skip = it > 0 ? 0u : OOB, sp = it > 0 ? so - 2 * BATCH_B : so;
      RR(0); RR(1); RR(2); RR(3); RR(4); RR(5);
      if (it > 0) b_chunk<0, 8, false>(K, V, q4, ok0, st4, r, mk);
      SCHED0();
      const unsigned sq2 = qseq++;
      if (c.wq == 0 && it > 0) lds_wait_ge4((volatile LAS v4u*)(fl + 32 + 4 * c.grp), (unsigned)it);
      q_arm<256>(c, qb_off + (unsigned)(b * 4 + 1) * Q_LAYER, sq2);
      RR(6); MAIN(0); MAIN(1);
      if (it > 0) b_chunk<8, 6, false>(K, V, q4, true, st4, r, mk);
      SCHED0();
      MAIN(2); MAIN(3); MAIN(4); MAIN(5); MAIN(6); MAIN(7);
      if (c.wq == 0) q_dma<256>(c, qb_off + (unsigned)(b * 4 + 1) * Q_LAYER);
      MAIN(8); MAIN(9);
      q_wait<256>(c, qb_off + (unsigned)(b * 4 + 1) * Q_LAYER, sq2);
      const v4u q2 = *(const LAS v4u*)(L + L_QB + c.grp * 1280 + 1024 + r * 16);
      SoftState st;
      b_chunk<MU(0), 8, true>(K, V, q2, ok0, st, r, mk);
      SCHED0();
      MAIN(10); MAIN(11);
      b_chunk<MU(4), 8, false>(K, V, q2, true, st, r, mk);
      SCHED0();
      MAIN(12); MAIN(13);
      b_chunk<MU(8), 8, false>(K, V, q2, true, st, r, mk);
      SCHED0();
      MAIN(14); MAIN(15); MAIN(16); MAIN(17);
      b_chunk<MU(12), 8, false>(K, V, q2, true, st, r, mk);
      SCHED0();
      MAIN(18);
      if (lane == 0) fl[c.wq] = (unsigned)(b + 1);
      b_chunk<MU(16), 4, false>(K, V, q2, true, st, r, mk);
      SCHED0();
      b_chunk<MU(18), 2, false>(K, V, q2, true, st, r, mk);
      b_publish(c, b, 1, st, (unsigned)(b < 2 ? b + 1 : 2 * b - 1));
      if (it > 0) b_publish(c, b - 2, 3, st4, (unsigned)(2 * b));
    }
    {
      const unsigned sq = qseq++;
      q_arm<1024>(c, qa_off + (unsigned)(b * 4 + 2) * Q_LAYER, sq);
      q_wait<1024>(c, qa_off + (unsigned)(b * 4 + 2) * Q_LAYER, sq);
      const AScores sc = a_qk(c, c.ldsA);
      a_pv(c, sc, c.ldsA, c.ldsA);
      if (c.lane == 0) fl[16 + 4 * c.grp + c.wq] = (unsigned)(it + 1);
      lds_wait_ge4((volatile LAS v4u*)(fl + 16 + 4 * c.grp), (unsigned)(it + 1));
      a_merge<false>(c, b, 2);
      if (c.lane == 0) fl[24 + 4 * c.grp + c.wq] = (unsigned)(it + 1);
    }
    if (it < 3) {
      lds_wait_ge4((volatile LAS v4u*)(fl + 24 + 4 * c.grp), (unsigned)(it + 1));
      wait_vm<0>();
      const unsigned sq = qseq++;
      q_arm<1024>(c, qa_off + (unsigned)((b + 2) * 4) * Q_LAYER, sq);
      a_issue<MK_THR_A>(p, c, b + 2, c.ldsA);
      wait_vm<0>();
      A_PASS1(b + 2, sq, c.ldsA)
    }
    {
      const unsigned sq = qseq++;
      q_arm<256>(c, qb_off + (unsigned)(b * 4 + 3) * Q_LAYER, sq);
      q_wait<256>(c, qb_off + (unsigned)(b * 4 + 3) * Q_LAYER, sq);
      const int lane = opaque(c.lane), r = lane & 15;
      const v4u mk = pv_masks(lane);
      q4 = *(const LAS v4u*)(L + L_QB + c.grp * 1280 + 1024 + r * 16);
      wait_lgkm();
      if (lane == 0) fl[32 + 4 * c.grp + c.wq] = (unsigned)(it + 1);
      b_chunk<0, 2 * NS, true>(K, V, q4, true, st4, r, mk);
      SCHED0();
    }
  }
  {
    const int lane = opaque(c.lane), r = lane & 15;
    const v4u mk = pv_masks(lane);
    const unsigned voff = lane * 16, voff0 = voff | ((lane >> 4) >= rem ? OOB : 0u), skip = 0u;
    const bool ok0 = r >= 2 || (r == 0 && (lane >> 4) < rem);
    LAS unsigned char* box = L + L_SCR + c.wq * BOX;
    if (c.grp == 0) {
      for (int b = 6; b < 8; ++b) {
        const unsigned sp = (unsigned)b * BATCH_B + b_off;
        RR(0); RR(1); RR(2); RR(3); RR(4); RR(5); RR(6);
        if (b == 7) {
          lds_wait_ge(fl + 36 + c.wq, 4u);
          q4 = *(const LAS v4u*)(L + L_QB + 1280 + 1024 + r * 16);
          st4.m = -INFINITY; st4.l = 0.f; st4.a = f32x4{0.f, 0.f, 0.f, 0.f};
        }
        b_chunk<0, 2 * NO, false>(K, V, q4, ok0, st4, r, mk);
        if (b == 6) b_publish(c, 6, 3, st4, 15u);
      }
      *(LAS f32x4*)(box + lane * 16) = st4.a;
      *(LAS f32x2*)(box + 1024 + lane * 8) = f32x2{st4.m, st4.l};
      wait_lgkm();
      if (lane == 0) fl[40 + c.wq] = 1u;
    } else {
      lds_wait_ge(fl + 40 + c.wq, 1u);
      const f32x4 a2 = *(const LAS f32x4*)(box + lane * 16);
      const f32x2 ml = *(const LAS f32x2*)(box + 1024 + lane * 8);
      const float mn = fmx(st4.m, ml.x), e1 = ex2((st4.m - mn) * LOG2E), e2 = ex2((ml.x - mn) * LOG2E);
      st4.a = st4.a * e1 + a2 * e2; st4.l = st4.l * e1 + ml.y * e2; st4.m = mn;
      b_publish(c, 7, 3, st4, 16u);
    }
  }
#undef POFF
#undef RR
#undef MAIN
#undef MU
#undef A_PASS1
}

struct WSlice { unsigned w[96]; };

__device__ __forceinline__ void pack_store(const v4u a, const v4u b, int n, WSlice& W, LAS unsigned char* lw) {
  const unsigned av[4] = {a.x, a.y, a.z, a.w}, bv[4] = {b.x, b.y, b.z, b.w};
  unsigned o[8];
#pragma unroll
  for (int i = 0; i < 4; ++i) {
    o[2 * i] = __builtin_amdgcn_perm(bv[i], av[i], 0x05040100u);
    o[2 * i + 1] = __builtin_amdgcn_perm(bv[i], av[i], 0x07060302u);
  }
  if (n < 12) {
#pragma unroll
    for (int e = 0; e < 8; ++e) W.w[8 * n + e] = (unsigned)opaque((int)o[e]);
  } else {
    *(LAS v4u*)(lw + (2 * (n - 12)) * 8192) = v4u{o[0], o[1], o[2], o[3]};
    *(LAS v4u*)(lw + (2 * (n - 12) + 1) * 8192) = v4u{o[4], o[5], o[6], o[7]};
  }
}
template <bool GQA>
__device__ __forceinline__ void load_wo(const unsigned short* wo, int h0, int w, int lane, WSlice& W, LAS unsigned char* lw) {
  const rsrc_t rs = mkrs(wo, NH * DH * DM * 2);
  const unsigned base = (unsigned)(((h0 + (w >> 2)) * DH + 32 * (w & 3)) * (DM * 2));
#pragma unroll
  for (int g = 3; g >= 0; --g) {
    v4u raw[8];
#pragma unroll
    for (int pc = 0; pc < 4; ++pc)
#pragma unroll
      for (int e = 0; e < 2; ++e) {
        const int k = GQA ? 8 * (2 * e + (g >> 1)) + 4 * (g & 1) + pc : 8 * g + 2 * pc + e;
        raw[2 * pc + e] = ld_nt(rs, lane * 16, base + k * (DM * 2));
      }
    SCHED0();
#pragma unroll
    for (int pc = 0; pc < 4; ++pc) pack_store(raw[2 * pc], raw[2 * pc + 1], 4 * g + pc, W, lw);
    SCHED0();
  }
}
__device__ __forceinline__ void load_wq(const unsigned short* wq, int h0, int w, int lane, WSlice& W, LAS unsigned char* lw) {
  const rsrc_t rs = mkrs(wq, NH * DH * DM * 2);
  const unsigned voff = (unsigned)((lane >> 4) * (NH * DH * 2) + (lane & 15) * 16), base = (unsigned)(64 * w * (NH * DH * 2) + h0 * (DH * 2));
#pragma unroll
  for (int q = 3; q >= 0; --q) {
    v4u raw[8];
#pragma unroll
    for (int i = 0; i < 4; ++i)
#pragma unroll
      for (int e = 0; e < 2; ++e) {
        const int n = 4 * q + i, n2 = n >> 1, hd = n & 1;
        raw[2 * i + e] = ld_nt(rs, voff, base + 4 * (2 * n2 + e) * (NH * DH * 2) + hd * (DH * 2));
      }
    SCHED0();
#pragma unroll
    for (int i = 0; i < 4; ++i) pack_store(raw[2 * i], raw[2 * i + 1], 4 * q + i, W, lw);
    SCHED0();
  }
}

struct SvcCtx { rsrc_t rw; LAS unsigned char* L; int h0, j16, w, lane; const unsigned short* x0; unsigned short* out; unsigned char* ws; };

__device__ __forceinline__ void svc_s1(const SvcCtx& c, const WSlice& W, int b, int layer) {
  const int lane = opaque(c.lane), w = c.w, sg = lane >> 2, pc = lane & 3, tid = w * 64 + lane;
  const unsigned rec = rec_base(b, layer, c.h0 + (w >> 2));
  const int nsrc = (layer & 1) ? NSRC_B : NSRC_A;
  const bool v0 = sg < nsrc, v1 = sg + 16 < nsrc;
  const unsigned o0 = sg * REC_B, po = (4 * (w & 3) + pc) * 16;
  typedef unsigned v3u __attribute__((ext_vector_type(3)));
  v4u d[2]; v3u e[2];
  d[0] = v0 ? v4u{SENT, SENT, SENT, SENT} : v4u{0u, 0u, 0u, 0u}; e[0] = v0 ? v3u{SENT, SENT, SENT} : v3u{0xFF800000u, 0u, 0u};
  d[1] = v1 ? v4u{SENT, SENT, SENT, SENT} : v4u{0u, 0u, 0u, 0u}; e[1] = v1 ? v3u{SENT, SENT, SENT} : v3u{0xFF800000u, 0u, 0u};
  for (unsigned spins = 0; spins < SPIN_CAP; ++spins) {
    CFENCE();
#pragma unroll
    for (int i = 0; i < 2; ++i) {
      if (max(max(e[i].x, e[i].y), e[i].z) == SENT) e[i] = __builtin_amdgcn_raw_buffer_load_b96(c.rw, o0 + i * 16 * REC_B + 256, rec, 16);
      if (umax4(d[i]) == SENT) d[i] = ld_sc1(c.rw, o0 + i * 16 * REC_B + po, rec);
    }
    SCHED0();
    const unsigned mx = max(max(umax4(d[0]), umax4(d[1])), max(max(max(e[0].x, e[0].y), e[0].z), max(max(e[1].x, e[1].y), e[1].z)));
    if (all_lanes(mx != SENT)) break;
  }
  const LAS unsigned char* lw = c.L + S_W + tid * 16;
  v4u xl[4];
#pragma unroll
  for (int i = 0; i < 4; ++i) xl[i] = *(const LAS v4u*)(lw + i * 8192);
  const float m0 = __uint_as_float(e[0].x), m1 = __uint_as_float(e[1].x);
  float M = fmx(m0, m1);
  M = fmx(M, dppf<ROR4>(M)); M = fmx(M, dppf<XOR8>(M)); M = xrow_max(M);
  const float w0 = ex2((m0 - M) * LOG2E), w1 = ex2((m1 - M) * LOG2E);
  float Ls = w0 * __uint_as_float(e[0].y) + w1 * __uint_as_float(e[1].y);
  const float f0 = w0 * __uint_as_float(e[0].z), f1 = w1 * __uint_as_float(e[1].z);
  float o[8];
#pragma unroll
  for (int i = 0; i < 4; ++i) {
    const unsigned u0 = d[0][i], u1 = d[1][i];
    const h16x2 a0 = __builtin_bit_cast(h16x2, u0), a1 = __builtin_bit_cast(h16x2, u1);
    o[2 * i] = (float)a0.x * f0 + (float)a1.x * f1;
    o[2 * i + 1] = (float)a0.y * f0 + (float)a1.y * f1;
  }
  float oa = rs4(o[0], o[1], o[2], o[3]), ob = rs4(o[4], o[5], o[6], o[7]);
  Ls = xrow_sum(Ls);
  oa += dppf<ROR4>(oa); ob += dppf<ROR4>(ob); Ls += dppf<ROR4>(Ls);
  oa += dppf<XOR8>(oa); ob += dppf<XOR8>(ob); Ls += dppf<XOR8>(Ls);
  const float inv = __builtin_amdgcn_rcpf(Ls);
  const unsigned pr = pk2(oa * inv, ob * inv);
  float y[8];
#pragma unroll
  for (int i = 0; i < 8; ++i) y[i] = 0.f;
#pragma unroll
  for (int n = 12; n < 14; ++n) {
    const unsigned op = __builtin_amdgcn_readlane(pr, 16 * (n >> 2) + (n & 3));
#pragma unroll
    for (int i = 0; i < 8; ++i) y[i] = dot2(xl[2 * (n - 12) + (i >> 2)][i & 3], op, y[i]);
  }
  SCHED0();
#pragma unroll
  for (int i = 0; i < 4; ++i) xl[i] = *(const LAS v4u*)(lw + (4 + i) * 8192);
#pragma unroll
  for (int n = 0; n < 12; ++n) {
    const unsigned op = __builtin_amdgcn_readlane(pr, 16 * (n >> 2) + (n & 3));
#pragma unroll
    for (int i = 0; i < 8; ++i) y[i] = dot2(W.w[8 * n + i], op, y[i]);
  }
#pragma unroll
  for (int n = 14; n < 16; ++n) {
    const unsigned op = __builtin_amdgcn_readlane(pr, 16 * (n >> 2) + (n & 3));
#pragma unroll
    for (int i = 0; i < 8; ++i) y[i] = dot2(xl[2 * (n - 14) + (i >> 2)][i & 3], op, y[i]);
  }
  LAS float* red = (LAS float*)(c.L + S_RED);
  *(LAS f32x4*)(red + w * DM + lane * 8) = f32x4{y[0], y[1], y[2], y[3]};
  *(LAS f32x4*)(red + w * DM + lane * 8 + 4) = f32x4{y[4], y[5], y[6], y[7]};
  bar();
  {
    const int g = lane >> 4, cq = lane & 15;
    const LAS float* rp = red + 2 * g * DM + 64 * w + 4 * cq;
    const f32x4 s = *(const LAS f32x4*)(rp) + *(const LAS f32x4*)(rp + DM);
    const float v = rs4(s[0], s[1], s[2], s[3]);
    st_sc1_32(c.rw, (unsigned)((64 * w + 4 * cq + g) * 4), WS_Y + (unsigned)(((b * 4 + layer) * 16 + c.j16) * (DM * 4)), __float_as_uint(v));
  }
}

__device__ __forceinline__ unsigned x_poll(const SvcCtx& c, unsigned voff, unsigned xo, unsigned first) {
  unsigned v = first;
  for (unsigned spins = 0; !all_lanes(v != 0xFFFFu) && spins < SPIN_CAP; ++spins) {
    CFENCE();
    v = __builtin_amdgcn_raw_buffer_load_b16(c.rw, voff, xo, 16);
  }
  return v;
}

template <bool GATHER>
__device__ __forceinline__ void svc_s2(const SvcCtx& c, const WSlice& W, int b, int layer, unsigned xin = 0u, bool have_x = false) {
  const int lane = opaque(c.lane), w = c.w, g = lane >> 4, cq = lane & 15, tid = w * 64 + lane;
  const int col = 64 * w + 4 * cq + g;
  const unsigned xprev = WS_X + (unsigned)((b * 4 + layer - 1) * (DM * 2));
  unsigned xb;
  if (have_x) xb = xin;
  else if (!GATHER || layer == 0) xb = c.x0[b * DM + col];
  else xb = __builtin_amdgcn_raw_buffer_load_b16(c.rw, col * 2, xprev, 16);
  float xv;
  if constexpr (GATHER) {
    const unsigned yo = WS_Y + (unsigned)((b * 4 + layer) * 16 * (DM * 4));
    const unsigned vo = (unsigned)(4 * g * (DM * 4) + (64 * w + 4 * cq) * 4);
    v4u d[4];
#pragma unroll
    for (int i = 0; i < 4; ++i) d[i] = v4u{SENT, SENT, SENT, SENT};
    for (unsigned spins = 0; spins < SPIN_CAP; ++spins) {
      CFENCE();
#pragma unroll
      for (int i = 0; i < 4; ++i)
        if (umax4(d[i]) == SENT) d[i] = ld_sc1(c.rw, vo + i * (DM * 4), yo);
      SCHED0();
      if (all_lanes(max(max(umax4(d[0]), umax4(d[1])), max(umax4(d[2]), umax4(d[3]))) != SENT)) break;
    }
    if (layer > 0) xb = x_poll(c, col * 2, xprev, xb);
    const f32x4 s = (__builtin_bit_cast(f32x4, d[0]) + __builtin_bit_cast(f32x4, d[1])) + (__builtin_bit_cast(f32x4, d[2]) + __builtin_bit_cast(f32x4, d[3]));
    xb = f2bf(bf2f(xb) + rbf(rs4(s[0], s[1], s[2], s[3])));
    if (c.j16 == 0) {
      __builtin_amdgcn_raw_buffer_store_b16((unsigned short)min(xb, 0xFFFEu), c.rw, col * 2, WS_X + (unsigned)((b * 4 + layer) * (DM * 2)), 16);
    }
  }
  xv = bf2f(xb);
  CFENCE();
  const LAS unsigned char* lw = c.L + S_W + 65536 + tid * 16;
  v4u xl[4];
#pragma unroll
  for (int i = 0; i < 4; ++i) xl[i] = *(const LAS v4u*)(lw + i * 8192);
  const float piv = __builtin_bit_cast(float, __builtin_amdgcn_readfirstlane(__builtin_bit_cast(int, xv)));
  const float dv = xv - piv;
  const float s1 = xrow_sum(row_sum(dv)), s2 = xrow_sum(row_sum(dv * dv));
  LAS float* stt = (LAS float*)(c.L + S_ST);
  if (lane == 0) *(LAS f32x2*)(stt + 2 * w) = f32x2{piv + s1 * (1.f / 64.f), s2 - s1 * s1 * (1.f / 64.f)};
  bar();
  float mean, M2;
  {
    const f32x2 t = *(const LAS f32x2*)(stt + 2 * (lane & 7));
    float ms = t.x;
    ms += dppf<XOR1>(ms); ms += dppf<XOR2>(ms); ms += dppf<XOR7>(ms);
    mean = ms * 0.125f;
    const float dd = t.x - mean;
    M2 = fmaf(64.f * dd, dd, t.y);
    M2 += dppf<XOR1>(M2); M2 += dppf<XOR2>(M2); M2 += dppf<XOR7>(M2);
  }
  const float mb = rbf(mean), den = rbf(__builtin_amdgcn_sqrtf(rbf(rbf(fmx(M2, 0.f) * (1.f / DM)) + 1e-5f)));
  const float xn = rbf(xv - mb) * __builtin_amdgcn_rcpf(den);
  const unsigned pr = pk2(xn, dppf<XOR1>(xn));
  float y[2][8];
#pragma unroll
  for (int i = 0; i < 8; ++i) { y[0][i] = 0.f; y[1][i] = 0.f; }
#define QSTEP_L(N2, BASE)                                                                                   \
  {                                                                                                         \
    const unsigned xp = dppu<BCAST + 2 * (N2)>(pr);                                                         \
    _Pragma("unroll") for (int i = 0; i < 8; ++i) {                                                         \
      y[0][i] = dot2(xl[(i >> 2)][i & 3], xp, y[0][i]);                                                     \
      y[1][i] = dot2(xl[2 + (i >> 2)][i & 3], xp, y[1][i]);                                                 \
    }                                                                                                       \
  }
#define QSTEP_R(N2)                                                                                         \
  {                                                                                                         \
    const unsigned xp = dppu<BCAST + 2 * (N2)>(pr);                                                         \
    _Pragma("unroll") for (int i = 0; i < 8; ++i) {                                                         \
      y[0][i] = dot2(W.w[16 * (N2) + i], xp, y[0][i]);                                                      \
      y[1][i] = dot2(W.w[16 * (N2) + 8 + i], xp, y[1][i]);                                                  \
    }                                                                                                       \
  }
  QSTEP_L(6, 0)
  SCHED0();
#pragma unroll
  for (int i = 0; i < 4; ++i) xl[i] = *(const LAS v4u*)(lw + (4 + i) * 8192);
  QSTEP_R(0) QSTEP_R(1) QSTEP_R(2) QSTEP_R(3) QSTEP_R(4) QSTEP_R(5)
  QSTEP_L(7, 0)
#undef QSTEP_L
#undef QSTEP_R
  LAS float* red = (LAS float*)(c.L + S_RED2);
#pragma unroll
  for (int hd = 0; hd < 2; ++hd) {
    red[w * 256 + hd * DH + cq * 8 + g] = rs4(y[hd][0], y[hd][1], y[hd][2], y[hd][3]);
    red[w * 256 + hd * DH + cq * 8 + 4 + g] = rs4(y[hd][4], y[hd][5], y[hd][6], y[hd][7]);
  }
  bar();
  if (w < 2) {
    f32x2 t[8];
#pragma unroll
    for (int i = 0; i < 8; ++i) t[i] = *(const LAS f32x2*)(red + i * 256 + tid * 2);
    const f32x2 s = ((t[0] + t[1]) + (t[2] + t[3])) + ((t[4] + t[5]) + (t[6] + t[7]));
    st_sc1_32(c.rw, tid * 4, WS_Q + (unsigned)(((b * 4 + layer + 1) * NH + c.h0) * (DH * 2)), pk2(s.x, s.y));
  }
}

__device__ __forceinline__ void svc_final(const SvcCtx& c) {
  const int lane = opaque(c.lane), b = c.w, pj = lane >> 3, cq = lane & 7;
  const unsigned yo = WS_Y + (unsigned)((b * 4 + 3) * 16 * (DM * 4));
  const unsigned vo = (unsigned)(pj * (DM * 4) + (32 * c.j16 + 4 * cq) * 4);
  const int col = 32 * c.j16 + 4 * cq + (lane >> 4);
  const unsigned xb = x_poll(c, col * 2, WS_X + (unsigned)((b * 4 + 2) * (DM * 2)), 0xFFFFu);
  v4u d0, d1;
  for (unsigned spins = 0;; ++spins) {
    CFENCE();
    d0 = ld_sc1(c.rw, vo, yo); d1 = ld_sc1(c.rw, vo + 8 * (DM * 4), yo);
    if (all_lanes(max(umax4(d0), umax4(d1)) != SENT) || spins > SPIN_CAP) break;
  }
  const f32x4 s = __builtin_bit_cast(f32x4, d0) + __builtin_bit_cast(f32x4, d1);
  float v = rs4(s[0], s[1], s[2], s[3]);
  v += dppf<XOR8>(v);
  if ((lane & 8) == 0) c.out[b * DM + col] = (unsigned short)f2bf(bf2f(xb) + rbf(v));
}

__device__ __forceinline__ void service_role(const Params& p, LAS unsigned char* L, int x, int slot, int w) {
  SvcCtx c;
  c.lane = opaque(lane_id()); c.w = w;
  const int tid = w * 64 + c.lane, set = slot >> 1;
  c.rw = mkrs(p.ws, WS_END); c.L = L; c.h0 = 4 * x + 2 * (slot & 1); c.j16 = 2 * x + (slot & 1); c.x0 = p.x0; c.out = p.out; c.ws = p.ws;
  const unsigned tag = arm_buffers(c.rw, tid);
  WSlice Wo, Wq;
  LAS unsigned char* lw = L + S_W + tid * 16;
  if (set == 0) {
    const int col = 64 * w + 4 * (c.lane & 15) + (c.lane >> 4);
    const unsigned xa = p.x0[col], xb = p.x0[DM + col];
    load_wq(p.wqA, c.h0, w, c.lane, Wq, lw + 65536);
    armed_wait(c.rw, tag, c.lane);
    if (blockIdx.x == 0 && tid == 0) __builtin_amdgcn_raw_buffer_store_b32(tag, c.rw, 0, WS_EPOCH, 16);
#pragma unroll 1
    for (int b = 0; b < 2; ++b) svc_s2<false>(c, Wq, b, -1, b ? xb : xa, true);
    load_wo<false>(p.woB, c.h0, w, c.lane, Wo, lw);
  } else {
    load_wo<true>(p.woA, c.h0, w, c.lane, Wo, lw);
    load_wq(p.wqB, c.h0, w, c.lane, Wq, lw + 65536);
    armed_wait(c.rw, tag, c.lane);
  }
  for (int i = 0; i < 2 * NB; ++i) {
    int b, second;
    if (i < 2) { b = i; second = 0; }
    else if (i >= 2 * NB - 2) { b = i - NB; second = 1; }
    else if (set == 0) { second = (i & 1); b = second ? (i - 3) >> 1 : (i + 2) >> 1; }
    else { second = !(i & 1); b = second ? (i - 2) >> 1 : (i + 1) >> 1; }
    const int layer = 2 * second + 1 - set;
    svc_s1(c, Wo, b, layer);
    if (layer < 3) svc_s2<true>(c, Wq, b, layer);
    else bar();
    if (set == 0 && layer == 1 && b + 2 < NB) svc_s2<false>(c, Wq, b + 2, -1);
  }
  if (set == 0) svc_final(c);
  bar();
}

__global__ void __launch_bounds__(NTHR, 2) k_mega(Params p) {
  extern __shared__ __attribute__((aligned(16))) unsigned char smem[];
  LAS unsigned char* L = (LAS unsigned char*)smem;
  const int x = blockIdx.x & 7, slot = blockIdx.x >> 3, w = __builtin_amdgcn_readfirstlane(threadIdx.x >> 6);
  if (slot < 4) service_role(p, L, x, slot, w);
  else stream_role(p, L, x, slot - 4, w);
}
}

extern "C" void kernel_launch(void* const* d_in, const int* in_sizes, int n_in, void* d_out, int out_size, void* d_ws, size_t ws_size, hipStream_t stream) {
  using namespace mk;
  Params p;
  p.x0 = (const unsigned short*)d_in[0];
  p.kA = (const unsigned short*)d_in[1]; p.vA = (const unsigned short*)d_in[2]; p.wqA = (const unsigned short*)d_in[3]; p.woA = (const unsigned short*)d_in[4];
  p.kB = (const unsigned short*)d_in[6]; p.vB = (const unsigned short*)d_in[7]; p.wqB = (const unsigned short*)d_in[8]; p.woB = (const unsigned short*)d_in[9];
  p.out = (unsigned short*)d_out;
  p.ws = (unsigned char*)d_ws;
  (void)hipFuncSetAttribute((const void*)k_mega, hipFuncAttributeMaxDynamicSharedMemorySize, LDS_BYTES);
  k_mega<<<NWG, NTHR, LDS_BYTES, stream>>>(p);
}
```

```cpp
#include <hip/hip_runtime.h>
#include <hip/hip_bf16.h>
#include <stdint.h>

namespace mk {
typedef unsigned v4u __attribute__((ext_vector_type(4)));
typedef unsigned v2u __attribute__((ext_vector_type(2)));
typedef float f32x4 __attribute__((ext_vector_type(4)));
typedef float f32x2 __attribute__((ext_vector_type(2)));
typedef short s16x4 __attribute__((ext_vector_type(4)));
typedef __bf16 bf16x8 __attribute__((ext_vector_type(8)));
typedef __bf16 bf16x2 __attribute__((ext_vector_type(2)));
typedef __amdgpu_buffer_rsrc_t rsrc_t;
#define LAS __attribute__((address_space(3)))

constexpr int NB = 8, DM = 512, NH = 32, HKV = 8, SK = 4096, DH = 128;
constexpr float SCALE = 0.08838834764831845f;
constexpr float LOG2E = 1.4426950408889634f;
constexpr int NWG = 256, NTHR = 512;
constexpr int NSRC_A = 28, NSRC_B = 7;
constexpr int REC_B = 384;
constexpr unsigned SENT = 0xFFFFFFFFu;
constexpr unsigned SPIN_CAP = 1u << 16;
constexpr unsigned O_RA = 0, O_RB = 2 * NH * NSRC_A * REC_B, O_Y = O_RB + 2 * NH * NSRC_B * REC_B, O_Q = O_Y + 4 * 16 * DM * 4, O_X = O_Q + 4 * NH * DH * 2,
                   BLK = O_X + 4 * DM * 2, WS_ARM1 = 2 * BLK, WS_ARM = NB * BLK, WS_FLAG = (WS_ARM + 4095u) / 4096u * 4096u, WS_EPOCH = WS_FLAG + NWG * 64,
                   WS_END = WS_EPOCH + 128;
static_assert(BLK % 128 == 0, "line-aligned blocks");
constexpr int ARM_IT1 = (WS_ARM1 + 224 * 256 * 16 - 1) / (224 * 256 * 16), ARM_IT2 = (WS_ARM - WS_ARM1 + 224 * 256 * 16 - 1) / (224 * 256 * 16);
__device__ __forceinline__ unsigned rec_base(int b, int layer, int head) {
  return (unsigned)b * BLK + ((layer & 1) ? O_RB + (unsigned)(((layer >> 1) * NH + head) * (NSRC_B * REC_B)) : O_RA + (unsigned)(((layer >> 1) * NH + head) * (NSRC_A * REC_B)));
}
__host__ __device__ __forceinline__ unsigned q_base(int b, int layer) { return (unsigned)b * BLK + O_Q + (unsigned)layer * (NH * DH * 2); }
__device__ __forceinline__ unsigned y_base(int b, int layer) { return (unsigned)b * BLK + O_Y + (unsigned)layer * (16 * DM * 4); }
__device__ __forceinline__ unsigned x_base(int b, int layer) { return (unsigned)b * BLK + O_X + (unsigned)layer * (DM * 2); }
constexpr unsigned WS_DBG = 32u << 20;
#define MK_NS 14
constexpr int NS = MK_NS;
constexpr int NPMAX = 19, NO = NPMAX - (NS - 2);
constexpr int W0 = NO - 2, W1 = NO - 1;
#define MK_THR 5
#define MK_DELAY0 0
#define MK_PF_W 6
#define MK_POLLMASK 1
#define MK_POLLSKIP 0
#define MK_SPINSLEEP 1
#define MK_YDELAY 0
#define MK_LIGHTREC 3
#define MK_RR1 8
#define MK_THR_LO 5
#define MK_NLO 4
#define MK_THR_HI 5
#define MK_THR_A 20
#define MK_THR_A0 8
constexpr int THR = MK_THR;
constexpr int A_BYTES = 147 * 512, BOX = 2080;
constexpr int L_SCR = 2 * A_BYTES, L_QB = L_SCR + 4 * BOX, L_FLG = L_QB + 2 * 1280, L_BB = L_FLG + 256, L_STAMP = L_BB + 3 * 544, LDS_BYTES = 160 * 1024;
constexpr int S_W = 0, S_RED = 2 * 65536, S_RED2 = S_RED + 8 * DM * 4, S_ST = S_RED2 + 8 * 256 * 4;
static_assert(S_ST + 256 + 2048 <= LDS_BYTES && L_STAMP + 512 <= LDS_BYTES, "LDS map");

struct Params {
  const unsigned short *x0, *kA, *vA, *wqA, *woA, *kB, *vB, *wqB, *woB;
  unsigned short* out;
  unsigned char* ws;
};

__device__ __forceinline__ float bf2f(unsigned b) { return __builtin_bit_cast(float, b << 16); }
__device__ __forceinline__ unsigned f2bf(float f) { return (unsigned)__builtin_bit_cast(unsigned short, (__bf16)f); }
__device__ __forceinline__ float rbf(float f) { return bf2f(f2bf(f)); }
__device__ __forceinline__ unsigned pk2(float lo, float hi) { return __builtin_bit_cast(unsigned, bf16x2{(__bf16)lo, (__bf16)hi}); }
__device__ __forceinline__ float dot2(unsigned a, unsigned b, float c) {
  return __builtin_amdgcn_fdot2_f32_bf16(__builtin_bit_cast(bf16x2, a), __builtin_bit_cast(bf16x2, b), c, false);
}
template <int CTRL> __device__ __forceinline__ float dppf(float x) {
  return __builtin_bit_cast(float, __builtin_amdgcn_mov_dpp(__builtin_bit_cast(int, x), CTRL, 0xf, 0xf, true));
}
template <int CTRL> __device__ __forceinline__ unsigned dppu(unsigned x) { return (unsigned)__builtin_amdgcn_mov_dpp((int)x, CTRL, 0xf, 0xf, true); }
constexpr int XOR1 = 0xB1, XOR2 = 0x4E, XOR7 = 0x141, XOR8 = 0x128, ROR4 = 0x124, BCAST = 0x150;
__device__ __forceinline__ float fmx(float a, float b) { return __builtin_fmaxf(a, b); }
__device__ __forceinline__ float xrow_max(float x) {
  auto s = __builtin_amdgcn_permlane16_swap(__float_as_uint(x), __float_as_uint(x), false, false);
  x = fmx(__uint_as_float(s[0]), __uint_as_float(s[1]));
  auto t = __builtin_amdgcn_permlane32_swap(__float_as_uint(x), __float_as_uint(x), false, false);
  return fmx(__uint_as_float(t[0]), __uint_as_float(t[1]));
}
__device__ __forceinline__ float xrow_sum(float x) {
  auto s = __builtin_amdgcn_permlane16_swap(__float_as_uint(x), __float_as_uint(x), false, false);
  x = __uint_as_float(s[0]) + __uint_as_float(s[1]);
  auto t = __builtin_amdgcn_permlane32_swap(__float_as_uint(x), __float_as_uint(x), false, false);
  return __uint_as_float(t[0]) + __uint_as_float(t[1]);
}
__device__ __forceinline__ float rs4(float a, float b, float c, float d) {
  auto s = __builtin_amdgcn_permlane16_swap(__float_as_uint(a), __float_as_uint(b), false, false);
  auto t = __builtin_amdgcn_permlane16_swap(__float_as_uint(c), __float_as_uint(d), false, false);
  const float u = __uint_as_float(s[0]) + __uint_as_float(s[1]), v = __uint_as_float(t[0]) + __uint_as_float(t[1]);
  auto r = __builtin_amdgcn_permlane32_swap(__float_as_uint(u), __float_as_uint(v), false, false);
  return __uint_as_float(r[0]) + __uint_as_float(r[1]);
}
__device__ __forceinline__ float row_max(float x) {
  x = fmx(x, dppf<XOR1>(x)); x = fmx(x, dppf<XOR2>(x)); x = fmx(x, dppf<ROR4>(x)); return fmx(x, dppf<XOR8>(x));
}
__device__ __forceinline__ float row_sum(float x) {
  x += dppf<XOR1>(x); x += dppf<XOR2>(x); x += dppf<ROR4>(x); return x + dppf<XOR8>(x);
}
__device__ __forceinline__ float ex2(float x) { return __builtin_amdgcn_exp2f(x); }
__device__ __forceinline__ rsrc_t mkrs(const void* p, unsigned bytes) { return __builtin_amdgcn_make_buffer_rsrc((void*)p, 0, (int)bytes, 0x00020000); }
__device__ __forceinline__ v4u ld_nt(rsrc_t rs, unsigned voff, unsigned soff) { return __builtin_amdgcn_raw_buffer_load_b128(rs, voff, soff, 2); }
__device__ __forceinline__ v4u ld_sc1(rsrc_t rs, unsigned voff, unsigned soff) { return __builtin_amdgcn_raw_buffer_load_b128(rs, voff, soff, 16); }
__device__ __forceinline__ v2u ld_sc1_64(rsrc_t rs, unsigned voff, unsigned soff) { return __builtin_amdgcn_raw_buffer_load_b64(rs, voff, soff, 16); }
__device__ __forceinline__ unsigned nosent(unsigned x) { return x < 0xFFFFFFFEu ? x : 0xFFFFFFFEu; }
__device__ __forceinline__ void st_sc1(rsrc_t rs, unsigned voff, unsigned soff, v4u v) {
  v.x = nosent(v.x); v.y = nosent(v.y); v.z = nosent(v.z); v.w = nosent(v.w);
  __builtin_amdgcn_raw_buffer_store_b128(v, rs, voff + soff, 0, 16);
}
__device__ __forceinline__ void st_sc1_64(rsrc_t rs, unsigned voff, unsigned soff, v2u v) {
  v.x = nosent(v.x); v.y = nosent(v.y);
  __builtin_amdgcn_raw_buffer_store_b64(v, rs, voff, soff, 16);
}
__device__ __forceinline__ void st_sc1_32(rsrc_t rs, unsigned voff, unsigned soff, unsigned v) { __builtin_amdgcn_raw_buffer_store_b32(nosent(v), rs, voff, soff, 16); }
__device__ __forceinline__ unsigned umax4(v4u v) { return max(max(v.x, v.y), max(v.z, v.w)); }
__device__ __forceinline__ bool all_lanes(bool ok) { return __builtin_amdgcn_ballot_w64(!ok) == 0ull; }
#define CFENCE() asm volatile("" ::: "memory")
__device__ __forceinline__ int lane_id() { return (int)__builtin_amdgcn_mbcnt_hi(~0u, __builtin_amdgcn_mbcnt_lo(~0u, 0u)); }
__device__ __forceinline__ int opaque(int v) { asm volatile("" : "+v"(v)); return v; }
#define SCHED0() __builtin_amdgcn_sched_barrier(0)
template <int N> __device__ __forceinline__ void wait_vm() { asm volatile("s_waitcnt vmcnt(%0)" ::"n"(N) : "memory"); }
__device__ __forceinline__ void bar() { asm volatile("s_waitcnt lgkmcnt(0)\n\ts_barrier" ::: "memory"); }
__device__ __forceinline__ void wait_lgkm() { asm volatile("s_waitcnt lgkmcnt(0)" ::: "memory"); }
__device__ __forceinline__ f32x4 mfma16(unsigned a0, unsigned a1, unsigned b0, unsigned b1, f32x4 c) {
  return __builtin_amdgcn_mfma_f32_16x16x16bf16_1k(__builtin_bit_cast(s16x4, v2u{a0, a1}), __builtin_bit_cast(s16x4, v2u{b0, b1}), c, 0, 0, 0);
}
__device__ __forceinline__ f32x4 mfma32(v4u a, v4u b, f32x4 c) {
  return __builtin_amdgcn_mfma_f32_16x16x32_bf16(__builtin_bit_cast(bf16x8, a), __builtin_bit_cast(bf16x8, b), c, 0, 0, 0);
}
typedef _Float16 h16x2 __attribute__((ext_vector_type(2)));
__device__ __forceinline__ unsigned pkh(float lo, float hi) { return __builtin_bit_cast(unsigned, h16x2{(_Float16)lo, (_Float16)hi}); }
__device__ __forceinline__ f32x2 rec_scale(float amax) {
  unsigned e = __float_as_uint(amax) & 0x7F800000u;
  e = min(max(e, 0x01000000u), 0x7E000000u);
  return f32x2{__uint_as_float(e), __uint_as_float(0x7F000000u - e)};
}
__device__ __forceinline__ unsigned launch_tag(rsrc_t rw) {
  return __builtin_amdgcn_readfirstlane(__builtin_amdgcn_raw_buffer_load_b32(rw, 0, WS_EPOCH, 16)) * 2654435761u + 12345u;
}
template <int ITERS, unsigned LO, unsigned HI>
__device__ __forceinline__ void arm_stores(rsrc_t rw, int sidx, int t256) {
  const unsigned sv = (unsigned)opaque(-1);
#pragma unroll
  for (int i = 0; i < ITERS; ++i) {
    const unsigned off = LO + ((unsigned)(sidx * ITERS + i) * 256u + (unsigned)t256) * 16u;
    if (off < HI) __builtin_amdgcn_raw_buffer_store_b128(v4u{sv, sv, sv, sv}, rw, off, 0, 16);
  }
}
__device__ __forceinline__ void armed_wait(rsrc_t rw, unsigned tag, unsigned stage, int lane) {
  for (unsigned spins = 0; spins < SPIN_CAP; ++spins) {
    CFENCE();
    bool ok = true;
#pragma unroll
    for (int i = 0; i < 4; ++i) {
      const unsigned d = __builtin_amdgcn_raw_buffer_load_b32(rw, (unsigned)min(i * 64 + lane, 223) * 64u, WS_FLAG, 16) - tag;
      ok &= d >= stage && d <= 1u;
    }
    if (all_lanes(ok)) break;
    __builtin_amdgcn_s_sleep(4);
  }
  CFENCE();
}
__device__ __forceinline__ void lds_wait_ge(volatile LAS unsigned* f, unsigned v) {
  for (unsigned spins = 0; *f < v && spins < (SPIN_CAP << 7); ++spins) __builtin_amdgcn_s_sleep(1);
  CFENCE();
}
__device__ __forceinline__ void lds_wait_ge_sleep(volatile LAS unsigned* f, unsigned v) {
  for (unsigned spins = 0; *f < v && spins < (SPIN_CAP << 6); ++spins) __builtin_amdgcn_s_sleep(2);
  CFENCE();
}
__device__ __forceinline__ void lds_wait_ge4(volatile LAS v4u* f, unsigned v) {
  for (unsigned spins = 0; spins < (SPIN_CAP << 8); ++spins) {
    const v4u t = *f;
    if (min(min(t.x, t.y), min(t.z, t.w)) >= v) break;
  }
  CFENCE();
}

struct SoftState { float m, l; f32x4 a; };

template <int U0, int NU>
__device__ __forceinline__ float b_scores(const v4u (&K)[2 * NS], const v4u q, int r) {
  constexpr int P2 = NU > 8 ? 16 : NU > 4 ? 8 : NU > 2 ? 4 : 2;
  float t[16];
#pragma unroll
  for (int u = 0; u < NU; ++u) {
    const v4u k = K[(U0 + u) % (2 * NS)];
    t[u] = dot2(k.w, q.w, dot2(k.z, q.z, dot2(k.y, q.y, dot2(k.x, q.x, 0.f))));
  }
#define TR_STEP(HALF, CTRL, CNT)                                                                  \
  _Pragma("unroll") for (int i = 0; i < (HALF); ++i) {                                            \
    if (i < (CNT)) {                                                                              \
      const float a_ = t[i] + dppf<CTRL>(t[i]);                                                   \
      if (i + (HALF) < (CNT)) { const float b_ = t[i + (HALF)] + dppf<CTRL>(t[i + (HALF)]); t[i] = (r & (HALF)) ? b_ : a_; } \
      else t[i] = a_;                                                                             \
    }                                                                                             \
  }
  if constexpr (P2 >= 16) { TR_STEP(8, XOR8, NU) }
  if constexpr (P2 >= 8) { TR_STEP(4, XOR7, (NU < 8 ? NU : 8)) }
  if constexpr (P2 >= 4) { TR_STEP(2, XOR2, (NU < 4 ? NU : 4)) }
  TR_STEP(1, XOR1, 2)
#undef TR_STEP
  float x = t[0];
  if constexpr (P2 < 4) x += dppf<XOR2>(x);
  if constexpr (P2 < 8) x += dppf<ROR4>(x);
  if constexpr (P2 < 16) x += dppf<XOR8>(x);
  return x;
}
template <int U0, int NU>
__device__ __forceinline__ f32x4 b_pv(const v4u (&V)[2 * NS], unsigned pp, const v4u mk, f32x4 a) {
#define PV(U)                                                                                     \
  if constexpr ((U) < NU) {                                                                       \
    const unsigned bc = dppu<BCAST + (U)>(pp);                                                    \
    a = mfma32(V[(U0 + (U)) % (2 * NS)], v4u{bc & mk.x, bc & mk.y, bc & mk.z, bc & mk.w}, a);     \
  }
  PV(0) PV(1) PV(2) PV(3) PV(4) PV(5) PV(6) PV(7) PV(8) PV(9) PV(10) PV(11) PV(12) PV(13) PV(14) PV(15)
#undef PV
  return a;
}
template <int U0, int NU, bool FIRST>
__device__ __forceinline__ void b_chunk(const v4u (&K)[2 * NS], const v4u (&V)[2 * NS], const v4u q, bool ok0, SoftState& st, int r, const v4u mk) {
  constexpr int N0 = NU > 16 ? 16 : NU, N1 = NU - N0;
  const float x0 = b_scores<U0, N0>(K, q, r);
  const float s0 = (r < N0 && ok0) ? rbf(rbf(x0) * SCALE) : -INFINITY;
  float s1 = -INFINITY;
  if constexpr (N1 > 0) {
    const float x1 = b_scores<U0 + 16, N1>(K, q, r);
    s1 = r < N1 ? rbf(rbf(x1) * SCALE) : -INFINITY;
  }
  float mn = xrow_max(row_max(fmx(s0, s1)));
  if constexpr (!FIRST) mn = fmx(mn, st.m);
  const float p0 = ex2((s0 - mn) * LOG2E);
  if constexpr (FIRST) {
    st.l = p0;
    st.a = f32x4{0.f, 0.f, 0.f, 0.f};
  } else {
    const float alpha = ex2((st.m - mn) * LOG2E);
    st.l = st.l * alpha + p0;
    st.a *= alpha;
  }
  st.m = mn;
  st.a = b_pv<U0, N0>(V, pk2(p0, p0), mk, st.a);
  if constexpr (N1 > 0) {
    const float p1 = ex2((s1 - mn) * LOG2E);
    st.l += p1;
    st.a = b_pv<U0 + 16, N1>(V, pk2(p1, p1), mk, st.a);
  }
  asm volatile("" : "+v"(st.l));
}
__device__ __forceinline__ v4u pv_masks(int lane) {
  const unsigned h = (lane & 1) ? 0xFFFF0000u : 0xFFFFu;
  const int t = (lane >> 1) & 3;
  return v4u{t == 0 ? h : 0u, t == 1 ? h : 0u, t == 2 ? h : 0u, t == 3 ? h : 0u};
}

template <int SLOT, int TH>
__device__ __forceinline__ void issue_pair(v4u (&K)[2 * NS], v4u (&V)[2 * NS], rsrc_t rk, rsrc_t rv, unsigned va, unsigned vb, unsigned soff) {
  wait_vm<TH>();
  K[2 * SLOT] = ld_nt(rk, va, soff);
  SCHED0();
  wait_vm<TH>();
  K[2 * SLOT + 1] = ld_nt(rk, vb + 1024, soff);
  SCHED0();
  wait_vm<TH>();
  V[2 * SLOT] = ld_nt(rv, va, soff);
  SCHED0();
  wait_vm<TH>();
  V[2 * SLOT + 1] = ld_nt(rv, vb + 1024, soff);
  SCHED0();
}

struct StreamCtx {
  rsrc_t rw;
  v4u rwv;
  LAS unsigned char* L;
  int lane, grp, wq, x, head, c28, widx;
  int nk;
  unsigned ldsA;
  unsigned a_off, a_tail;
};
__device__ __forceinline__ volatile LAS unsigned* flags(const StreamCtx& c) { return (volatile LAS unsigned*)(c.L + L_FLG); }
__device__ __forceinline__ unsigned a_region(int grp, int c28, int wq) {
  return (unsigned)(grp * A_BYTES + (((c28 * 4 + wq) * 4096) / 112 - (c28 * 16384) / 112) * 512);
}

template <int TH>
__device__ __forceinline__ void a_issue(const Params& p, const StreamCtx& c, int b, unsigned region) {
  const int lane = opaque(c.lane), a = lane >> 4, pos = lane & 15;
  const rsrc_t rk = mkrs(p.kA, NB * HKV * SK * DH * 2), rv = mkrs(p.vA, NB * HKV * SK * DH * 2);
  const unsigned so = (unsigned)b * (HKV * SK * DH * 2) + c.a_off;
  const unsigned kv = a * 256 + ((pos ^ (4 * a)) * 16), vv = lane * 16;
  LAS unsigned char* kd = c.L + region;
  LAS unsigned char* vd = kd + c.nk * 256;
  if (c.nk == 37 && lane < 16) {
    __builtin_amdgcn_raw_ptr_buffer_load_lds(rk, (LAS void*)(kd + 9 * 1024), 16, kv ^ 16, so + c.a_tail, 0, 2);
    __builtin_amdgcn_raw_ptr_buffer_load_lds(rv, (LAS void*)(vd + 9 * 1024), 16, vv, so + c.a_tail, 0, 2);
  }
#pragma unroll
  for (int u = 0; u < 9; ++u) {
    wait_vm<TH>();
    __builtin_amdgcn_raw_ptr_buffer_load_lds(rk, (LAS void*)(kd + u * 1024), 16, kv ^ ((u & 3) * 16), so + u * 4096, 0, 2);
    __builtin_amdgcn_raw_ptr_buffer_load_lds(rv, (LAS void*)(vd + u * 1024), 16, vv, so + u * 4096, 0, 2);
  }
}

__device__ __forceinline__ void b_publish(const StreamCtx& c, int b, int layer, const SoftState& st, unsigned seq, unsigned bbase = L_BB, int f0 = 44, int bstr = 544) {
  const int lane = opaque(c.lane), cc = lane & 15, lg = lane >> 4;
  volatile LAS unsigned* fl = flags(c) + (f0 - 44);
  float l = xrow_sum(row_sum(st.l)), m = st.m;
  const unsigned pos = (unsigned)(lg * 8 + (cc & 7)) * 16u;
  if (c.wq < 3) {
    LAS unsigned char* o = c.L + bbase + c.wq * bstr;
    lds_wait_ge(fl + 47, seq - 1);
    if (cc < 8) *(LAS f32x4*)(o + pos) = st.a;
    if (lane == 0) *(LAS f32x2*)(o + 512) = f32x2{m, l};
    wait_lgkm();
    if (lane == 0) fl[44 + c.wq] = seq;
  } else {
    for (unsigned spins = 0; spins < (SPIN_CAP << 8); ++spins) {
      const v4u t = *(volatile LAS v4u*)(fl + 44);
      if (min(min(t.x, t.y), t.z) >= seq) break;
    }
    CFENCE();
    f32x4 a[3]; f32x2 ml[3];
#pragma unroll
    for (int i = 0; i < 3; ++i) { a[i] = *(const LAS f32x4*)(c.L + bbase + i * bstr + pos); ml[i] = *(const LAS f32x2*)(c.L + bbase + i * bstr + 512); }
    const float M = fmx(fmx(m, ml[0].x), fmx(ml[1].x, ml[2].x));
    const float e = ex2((m - M) * LOG2E);
    f32x4 o = st.a * e;
    l *= e;
#pragma unroll
    for (int i = 0; i < 3; ++i) { const float ei = ex2((ml[i].x - M) * LOG2E); o += a[i] * ei; l = fmaf(ml[i].y, ei, l); }
    CFENCE();
    if (lane == 0) fl[47] = seq;
    const f32x2 sc = rec_scale(xrow_max(row_max(fmx(fmx(fabsf(o[0]), fabsf(o[1])), fmx(fabsf(o[2]), fabsf(o[3]))))));
    o *= sc.y;
    const unsigned ro = rec_base(b, layer, c.head) + (unsigned)((c.c28 % 7) * REC_B);
    if (cc < 8) st_sc1_64(c.rw, pos >> 1, ro, v2u{pkh(o[0], o[1]), pkh(o[2], o[3])});
    if (lane == 8) st_sc1(c.rw, 256, ro, v4u{__float_as_uint(M), __float_as_uint(l), __float_as_uint(sc.x), 0u});
  }
  CFENCE();
}

template <bool SHARED>
__device__ __forceinline__ void a_merge(const StreamCtx& c, int b, int layer) {
  const int lane = opaque(c.lane), kg = lane >> 4, col = lane & 15, wq = c.wq;
  unsigned src;
  if constexpr (SHARED) src = (unsigned)(L_SCR + kg * BOX);
  else {
    const unsigned r0 = a_region(c.grp, c.c28, 0), r1 = a_region(c.grp, c.c28, 1), r2 = a_region(c.grp, c.c28, 2), r3 = a_region(c.grp, c.c28, 3);
    src = (kg & 2) ? ((kg & 1) ? r3 : r2) : ((kg & 1) ? r1 : r0);
  }
  const LAS unsigned char* in = c.L + src;
  const f32x2 ml = *(const LAS f32x2*)(in + 2048 + wq * 8);
  f32x4 b0 = *(const LAS f32x4*)(in + (wq * 16 + col) * 32), b1 = *(const LAS f32x4*)(in + (wq * 16 + col) * 32 + 16);
  const float M = xrow_max(ml.x), e = ex2((ml.x - M) * LOG2E);
  b0 *= e; b1 *= e;
  const float Lt = xrow_sum(ml.y * e);
  const float o0 = rs4(b0[0], b0[1], b0[2], b0[3]), o1 = rs4(b1[0], b1[1], b1[2], b1[3]);
  const f32x2 sc = rec_scale(xrow_max(row_max(fmx(fabsf(o0), fabsf(o1)))));
  const unsigned ro = rec_base(b, layer, 4 * c.x + wq) + (unsigned)(c.c28 * REC_B);
  st_sc1_32(c.rw, (unsigned)(col * 16 + kg * 4), ro, pkh(o0 * sc.y, o1 * sc.y));
  if (lane == 0) st_sc1(c.rw, 256, ro, v4u{__float_as_uint(M), __float_as_uint(Lt), __float_as_uint(sc.x), 0u});
  CFENCE();
}
template <int NBYTES>
__device__ __forceinline__ void q_dma(const StreamCtx& c, unsigned qoff) {
  const unsigned dst = (unsigned)(size_t)(c.L + L_QB + c.grp * 1280 + (NBYTES == 1024 ? 0 : 1024));
  const unsigned voff = opaque(c.lane) * 16;
  if (voff < NBYTES) {
    unsigned keep;
    asm volatile("s_mov_b32 %0, m0\n\ts_mov_b32 m0, %1\n\ts_nop 0\n\tbuffer_load_dwordx4 %2, %3, %4 offen sc1 lds\n\ts_mov_b32 m0, %0"
                 : "=&s"(keep) : "s"(dst), "v"(voff), "s"(c.rwv), "s"(qoff) : "memory");
  }
}
template <int NBYTES>
__device__ __forceinline__ void q_arm(const StreamCtx& c, unsigned qoff, unsigned seq) {
  if (c.wq == 0) {
    const int lane = opaque(c.lane);
    const unsigned sv = (unsigned)opaque(-1);
    *(LAS v4u*)(c.L + L_QB + c.grp * 1280 + (NBYTES == 1024 ? 0 : 1024) + ((lane * 16) & (NBYTES - 1))) = v4u{sv, sv, sv, sv};
    wait_lgkm();
    if (lane == 0) flags(c)[12 + c.grp] = seq;
    q_dma<NBYTES>(c, qoff);
  }
}
template <int NBYTES>
__device__ __forceinline__ void q_wait(const StreamCtx& c, unsigned qoff, unsigned seq) {
  const int lane = opaque(c.lane);
  volatile LAS v4u* qp = (volatile LAS v4u*)(c.L + L_QB + c.grp * 1280 + (NBYTES == 1024 ? 0 : 1024) + ((lane * 16) & (NBYTES - 1)));
  volatile LAS unsigned* armed = flags(c) + 12 + c.grp;
  for (unsigned spins = 0; spins < (SPIN_CAP << 6); ++spins) {
    const unsigned a = *armed;
    const v4u q = *qp;
    if (all_lanes(a >= seq && umax4(q) != SENT)) break;
    if (c.wq == 0 && (spins & MK_POLLMASK) == MK_POLLMASK && spins >= MK_POLLSKIP) q_dma<NBYTES>(c, qoff);
    __builtin_amdgcn_s_sleep(MK_SPINSLEEP);
  }
  CFENCE();
}

struct AScores { float s[10]; };
__device__ __forceinline__ AScores a_qk(const StreamCtx& c, unsigned region) {
  const int lane = opaque(c.lane), kg = lane >> 4, col = lane & 15, hh = col >> 2;
  const LAS unsigned char* base = c.L + region;
  const LAS unsigned char* qb = c.L + L_QB + c.grp * 1280 + hh * 256 + kg * 16;
  f32x4 sc[3] = {{0.f, 0.f, 0.f, 0.f}, {0.f, 0.f, 0.f, 0.f}, {0.f, 0.f, 0.f, 0.f}};
  const unsigned ro = (unsigned)((col & 3) * 1024 + (col >> 2) * 256);
#pragma unroll
  for (int th = 0; th < 2; ++th) {
    v4u qf[2], kf[2][3];
#pragma unroll
    for (int t2 = 0; t2 < 2; ++t2) {
      const int t = 2 * th + t2;
      const LAS unsigned char* kp = base + ro + 16 * ((4 * t + kg) ^ col);
      qf[t2] = *(const LAS v4u*)(qb + t * 64);
#pragma unroll
      for (int tau = 0; tau < 3; ++tau) kf[t2][tau] = *(const LAS v4u*)(kp + tau * 4096);
    }
    SCHED0();
#pragma unroll
    for (int t2 = 0; t2 < 2; ++t2)
#pragma unroll
      for (int tau = 0; tau < 3; ++tau) sc[tau] = mfma32(kf[t2][tau], qf[t2], sc[tau]);
  }
  AScores o;
#pragma unroll
  for (int u = 0; u < 10; ++u) o.s[u] = rbf(rbf(sc[u >> 2][u & 3]) * SCALE);
  if (!(c.nk == 37 && kg == 0)) o.s[9] = -INFINITY;
  return o;
}
__device__ __forceinline__ void a_pv(const StreamCtx& c, const AScores& sc, unsigned region, unsigned box) {
  const int lane = opaque(c.lane), kg = lane >> 4, col = lane & 15, hh = col >> 2;
  const unsigned m0 = (lane & 3) == 0 ? 0xFFFFu : (lane & 3) == 1 ? 0xFFFF0000u : 0u;
  const unsigned m1 = (lane & 3) == 2 ? 0xFFFFu : (lane & 3) == 3 ? 0xFFFF0000u : 0u;
  const LAS unsigned char* vbase = c.L + region + c.nk * 256 + lane * 16;
  float mx = sc.s[0];
#pragma unroll
  for (int u = 1; u < 10; ++u) mx = fmx(mx, sc.s[u]);
  const float m = xrow_max(mx), mneg = -m * LOG2E;
  float l = 0.f;
  f32x4 a0 = {0.f, 0.f, 0.f, 0.f}, a1 = {0.f, 0.f, 0.f, 0.f};
#pragma unroll
  for (int u = 0; u < 10; ++u) {
    if (u < 9 || c.nk == 37) {
      const float pr = ex2(fmaf(sc.s[u], LOG2E, mneg));
      l += pr;
      const unsigned pp = pk2(pr, pr), b0 = pp & m0, b1 = pp & m1;
      v4u v = *(const LAS v4u*)(vbase + u * 1024);
      if (u == 9 && kg != 0) v = v4u{0u, 0u, 0u, 0u};
      a0 = mfma16(v.x, v.y, b0, b1, a0);
      a1 = mfma16(v.z, v.w, b0, b1, a1);
    }
  }
  l = xrow_sum(l);
  LAS unsigned char* o = c.L + box;
  const unsigned id = (unsigned)(hh * 16 + kg * 4 + (col & 3));
  *(LAS f32x4*)(o + id * 32) = a0;
  *(LAS f32x4*)(o + id * 32 + 16) = a1;
  if ((lane & 0x33) == 0) *(LAS f32x2*)(o + 2048 + hh * 8) = f32x2{m, l};
  wait_lgkm();
}

__device__ __forceinline__ void stream_role(const Params& p, LAS unsigned char* L, int x, int c28, int w) {
  StreamCtx c;
  const int tid = w * 64 + opaque(lane_id());
  c.rw = mkrs(p.ws, WS_END);
  c.rwv = v4u{(unsigned)(size_t)p.ws, (unsigned)((size_t)p.ws >> 32) & 0xFFFFu, WS_END, 0x00020000u};
  c.L = L; c.lane = tid & 63; c.grp = w >> 2; c.wq = w & 3; c.x = x; c.c28 = c28;
  c.head = 4 * x + c28 / 7;
  c.widx = (c28 % 7) * 4 + c.wq;
  const int a112 = c28 * 4 + c.wq, ka0 = (a112 * 4096) / 112;
  c.nk = ((a112 + 1) * 4096) / 112 - ka0;
  c.ldsA = a_region(c.grp, c28, c.wq);
  const int ka_cu = (c28 * 16384) / 112;
  c.a_off = (unsigned)((x * SK + ka_cu) * (DH * 2)) + (unsigned)c.wq * 1024u;
  c.a_tail = (unsigned)((144 + (ka0 - ka_cu) - 36 * c.wq) * (DH * 2)) - (unsigned)c.wq * 1024u;
  const int c7 = c28 % 7, kb0 = (c7 * 4096) / 7, ntail = ((c7 + 1) * 4096) / 7 - kb0 - 576;
  const int t0 = (c.wq * ntail) / 4, rem = ((c.wq + 1) * ntail) / 4 - t0;
  const unsigned b_off = (unsigned)((c.head * SK + kb0) * (DH * 2)) + (unsigned)c.wq * 2048u;
  const unsigned p0_off = (unsigned)((576 + t0) * (DH * 2)) - (unsigned)c.wq * 2048u;
  constexpr unsigned BATCH_B = NH * SK * DH * 2, OOB = 0x40000000u;
  const rsrc_t rk = mkrs(p.kB, NB * BATCH_B), rv = mkrs(p.vB, NB * BATCH_B);
  const unsigned qa_off = (unsigned)(4 * x * DH * 2), qb_off = (unsigned)(c.head * DH * 2);
  volatile LAS unsigned* fl = flags(c);
  if (tid < 64) ((LAS unsigned*)(L + L_FLG))[tid] = 0u;
  bar();

  v4u K[2 * NS], V[2 * NS];
  SoftState st4 = {-INFINITY, 0.f, {0.f, 0.f, 0.f, 0.f}};
  v4u q4 = {0u, 0u, 0u, 0u};
  unsigned qseq = 1;

#define POFF(i) ((i) == 0 ? p0_off : (unsigned)((i)-1) * 8192u)
#define RRS(i, slot) issue_pair<(slot), THR>(K, V, rk, rv, (i) == 0 ? voff0 : voff, (i) == 0 ? OOB : voff, sp + POFF(i))
#define MAIN(i) issue_pair<(i) % NS, THR>(K, V, rk, rv, (i) == 0 ? voff0 : voff, (i) == 0 ? OOB : voff, so + POFF(i))
#define MU(i) (2 * ((i) % NS))
#define LANE_CONSTS                                                                  \
  const int lane = opaque(c.lane), r = lane & 15;                                    \
  const v4u mk = pv_masks(lane);                                                     \
  const unsigned voff = lane * 16, voff0 = voff | ((lane >> 4) >= rem ? OOB : 0u);   \
  const bool ok0 = r >= 2 || (r == 0 && (lane >> 4) < rem);
#define RR_STEP(i, last)                                                             \
  if constexpr ((i) <= (last)) { b_chunk<2 * W0, 2, false>(K, V, q4, (i) == 0 ? ok0 : true, st4, r, mk); SCHED0(); } \
  if constexpr ((i) + 2 <= (last)) { RRS((i) + 2, W0); }                             \
  if constexpr ((i) + 1 <= (last)) { b_chunk<2 * W1, 2, false>(K, V, q4, true, st4, r, mk); SCHED0(); } \
  if constexpr ((i) + 3 <= (last)) { RRS((i) + 3, W1); }
#define A_PASS1(bb, sq_, reg_)                                                        \
  {                                                                                 \
    q_wait<1024>(c, qa_off + q_base(bb, 0), sq_);                  \
    const AScores sc = a_qk(c, reg_);                                               \
    lds_wait_ge4((volatile LAS v4u*)(fl + 4), (unsigned)(bb));                      \
    if ((bb) > 4) lds_wait_ge(fl + 51, (unsigned)((bb)-4));                         \
    a_pv(c, sc, reg_, (unsigned)(L_SCR + c.wq * BOX));                              \
    if (c.lane == 0) fl[8 + c.wq] = (unsigned)((bb) + 1);                           \
    lds_wait_ge4((volatile LAS v4u*)(fl + 8), (unsigned)((bb) + 1));                \
    a_merge<true>(c, bb, 0);                                                        \
    if (c.lane == 0) fl[4 + c.wq] = (unsigned)((bb) + 1);                           \
    SCHED0();                                                                       \
  }
  static_assert(NS == 14, "static schedule below");
  constexpr int RR1 = MK_RR1 < NO - 1 ? MK_RR1 : NO - 1;

  if (c.grp == 1) {
    const unsigned tag = launch_tag(c.rw);
#pragma unroll 1
    for (int bb = 0; bb < 2; ++bb) {
      a_issue<MK_THR_A0>(p, c, bb, c.ldsA - (1 - bb) * A_BYTES);
      wait_vm<0>();
      if (bb == 0 && c.wq == 0) armed_wait(c.rw, tag, 0u, opaque(c.lane));
      const unsigned sq = qseq++;
      q_arm<1024>(c, qa_off + q_base(bb, 0), sq);
      A_PASS1(bb, sq, c.ldsA - (1 - bb) * A_BYTES)
    }
    arm_stores<ARM_IT2, WS_ARM1, WS_ARM>(c.rw, c28 * 8 + x, c.wq * 64 + opaque(c.lane));
    wait_vm<0>();
    if (c.lane == 0) __hip_atomic_fetch_add((LAS unsigned*)(L + L_FLG) + 57, 1u, __ATOMIC_RELAXED, __HIP_MEMORY_SCOPE_WORKGROUP);
    if (c.wq == 3) {
      lds_wait_ge(fl + 57, 4u);
      if (c.lane == 0) __builtin_amdgcn_raw_buffer_store_b32(tag + 1u, c.rw, (unsigned)(c28 * 8 + x) * 64u, WS_FLAG, 16);
    }
    if (c.wq == 0) {
      armed_wait(c.rw, tag, 1u, opaque(c.lane));
      if (c.lane == 0) fl[58] = 1u;
    }
  } else {
    const int lane = opaque(c.lane);
    const unsigned e0 = __builtin_amdgcn_raw_buffer_load_b32(c.rw, 0, WS_EPOCH, 16);
    arm_stores<ARM_IT1, 0u, WS_ARM1>(c.rw, c28 * 8 + x, c.wq * 64 + lane);
#pragma unroll
    for (int i = 0; i < 19; ++i) {
      const int ch = min(i * 112 + a112, 2047), m = ch >> 9, row = ch & 511;
      const unsigned short* src = (m & 1) ? (m == 1 ? p.woA : p.woB) + (size_t)(512 * x + row) * DM : (m == 0 ? p.wqA : p.wqB) + (size_t)row * (NH * DH) + 512 * x;
      if (i >= MK_PF_W) wait_vm<MK_PF_W>();
      if (i == MK_PF_W) {
        if (lane == 0) __hip_atomic_fetch_add((LAS unsigned*)(L + L_FLG) + 56, 1u, __ATOMIC_RELAXED, __HIP_MEMORY_SCOPE_WORKGROUP);
        if (c.wq == 3) {
          lds_wait_ge(fl + 56, 4u);
          if (lane == 0) __builtin_amdgcn_raw_buffer_store_b32(e0 * 2654435761u + 12345u, c.rw, (unsigned)(c28 * 8 + x) * 64u, WS_FLAG, 16);
        }
      }
      K[i] = __builtin_nontemporal_load((const v4u*)(src + lane * 8));
    }
#pragma unroll
    for (int i = 0; i < 19; ++i) asm volatile("" ::"v"(K[i]));
    if (c.wq == 0) armed_wait(c.rw, __builtin_amdgcn_readfirstlane(e0) * 2654435761u + 12345u, 0u, lane);
  }

  for (int it = 0; it < 4; ++it) {
    const int b = 2 * it + c.grp;
    const unsigned so = (unsigned)b * BATCH_B + b_off;
    {
      lds_wait_ge_sleep(fl + c.wq, (unsigned)b);
      LANE_CONSTS
      MAIN(0); MAIN(1); MAIN(2);
      const unsigned sq2 = qseq++;
      if (c.wq == 0 && it > 0) lds_wait_ge4((volatile LAS v4u*)(fl + 32 + 4 * c.grp), (unsigned)it);
      q_arm<256>(c, qb_off + q_base(b, 1), sq2);
      MAIN(3); MAIN(4); MAIN(5); MAIN(6); MAIN(7); MAIN(8); MAIN(9);
      if (c.wq == 0) q_dma<256>(c, qb_off + q_base(b, 1));
      MAIN(10); MAIN(11);
      q_wait<256>(c, qb_off + q_base(b, 1), sq2);
      const v4u q2 = *(const LAS v4u*)(L + L_QB + c.grp * 1280 + 1024 + r * 16);
      SoftState st;
      b_chunk<MU(0), 8, true>(K, V, q2, ok0, st, r, mk);
      SCHED0();
      MAIN(12); MAIN(13);
      b_chunk<MU(4), 8, false>(K, V, q2, true, st, r, mk);
      SCHED0();
      MAIN(14); MAIN(15);
      b_chunk<MU(8), 8, false>(K, V, q2, true, st, r, mk);
      SCHED0();
      MAIN(16); MAIN(17);
      b_chunk<MU(12), 8, false>(K, V, q2, true, st, r, mk);
      SCHED0();
      MAIN(18);
      if (lane == 0) fl[c.wq] = (unsigned)(b + 1);
      b_chunk<MU(16), 4, false>(K, V, q2, true, st, r, mk);
      SCHED0();
      b_chunk<MU(18), 2, false>(K, V, q2, true, st, r, mk);
      b_publish(c, b, 1, st, (unsigned)(b + 1));
    }
    const unsigned sp = so - 2 * BATCH_B;
    {
      const unsigned sq = qseq++;
      q_arm<1024>(c, qa_off + q_base(b, 2), sq);
      if (it > 0) {
        LANE_CONSTS
        RRS(0, W0); RRS(1, W1);
        RR_STEP(0, RR1) RR_STEP(2, RR1) RR_STEP(4, RR1) RR_STEP(6, RR1) RR_STEP(8, RR1)
      }
      q_wait<1024>(c, qa_off + q_base(b, 2), sq);
      const AScores sc = a_qk(c, c.ldsA);
      a_pv(c, sc, c.ldsA, c.ldsA);
      if (c.lane == 0) fl[16 + 4 * c.grp + c.wq] = (unsigned)(it + 1);
      lds_wait_ge4((volatile LAS v4u*)(fl + 16 + 4 * c.grp), (unsigned)(it + 1));
      a_merge<false>(c, b, 2);
      if (c.lane == 0) fl[24 + 4 * c.grp + c.wq] = (unsigned)(it + 1);
    }
    if (it < 3) {
      lds_wait_ge4((volatile LAS v4u*)(fl + 24 + 4 * c.grp), (unsigned)(it + 1));
      wait_vm<0>();
      if (b == 0 && c.wq == 0) lds_wait_ge(fl + 58, 1u);
      const unsigned sq = qseq++;
      q_arm<1024>(c, qa_off + q_base(b + 2, 0), sq);
      a_issue<MK_THR_A>(p, c, b + 2, c.ldsA);
      wait_vm<0>();
      A_PASS1(b + 2, sq, c.ldsA)
    }
    {
      const unsigned sq = qseq++;
      q_arm<256>(c, qb_off + q_base(b, 3), sq);
      LANE_CONSTS
      if (it > 0) {
        if constexpr (RR1 + 1 < NO) { RRS(RR1 + 1, W0); }
        if constexpr (RR1 + 2 < NO) { RRS(RR1 + 2, W1); }
        RR_STEP(RR1 + 1, NO - 1) RR_STEP(RR1 + 3, NO - 1) RR_STEP(RR1 + 5, NO - 1)
        lds_wait_ge4((volatile LAS v4u*)(fl + 4), (unsigned)min(b + 3, 8));
        b_publish(c, b - 2, 3, st4, (unsigned)(b - 1), (unsigned)(L_SCR + 1536), 48, BOX);
      }
      q_wait<256>(c, qb_off + q_base(b, 3), sq);
      q4 = *(const LAS v4u*)(L + L_QB + c.grp * 1280 + 1024 + r * 16);
      wait_lgkm();
      if (lane == 0) fl[32 + 4 * c.grp + c.wq] = (unsigned)(it + 1);
      b_chunk<2 * NO, 2 * (NS - 2), true>(K, V, q4, true, st4, r, mk);
      SCHED0();
    }
  }
  {
    LANE_CONSTS
    LAS unsigned char* box = L + L_SCR + c.wq * BOX;
    if (c.grp == 0) {
#pragma unroll 1
      for (int b = 6; b < 8; ++b) {
        const unsigned sp = (unsigned)b * BATCH_B + b_off;
        RRS(0, 0); RRS(1, 1); RRS(2, 2); RRS(3, 3); RRS(4, 4); RRS(5, 5); RRS(6, 6);
        if constexpr (NO > 7) { RRS(7, 7); }
        if constexpr (NO > 8) { RRS(8, 8); }
        if (b == 7) {
          lds_wait_ge(fl + 36 + c.wq, 4u);
          q4 = *(const LAS v4u*)(L + L_QB + 1280 + 1024 + r * 16);
          st4.m = -INFINITY; st4.l = 0.f; st4.a = f32x4{0.f, 0.f, 0.f, 0.f};
        }
        b_chunk<0, 2 * NO, false>(K, V, q4, ok0, st4, r, mk);
        if (b == 6) b_publish(c, 6, 3, st4, 1u, 4096u, 52);
      }
      *(LAS f32x4*)(box + lane * 16) = st4.a;
      *(LAS f32x2*)(box + 1024 + lane * 8) = f32x2{st4.m, st4.l};
      wait_lgkm();
      if (lane == 0) fl[40 + c.wq] = 1u;
    } else {
      lds_wait_ge(fl + 40 + c.wq, 1u);
      const f32x4 a2 = *(const LAS f32x4*)(box + lane * 16);
      const f32x2 ml = *(const LAS f32x2*)(box + 1024 + lane * 8);
      const float mn = fmx(st4.m, ml.x), e1 = ex2((st4.m - mn) * LOG2E), e2 = ex2((ml.x - mn) * LOG2E);
      st4.a = st4.a * e1 + a2 * e2; st4.l = st4.l * e1 + ml.y * e2; st4.m = mn;
      b_publish(c, 7, 3, st4, 9u);
    }
  }
#undef POFF
#undef RRS
#undef RR_STEP
#undef LANE_CONSTS
#undef MAIN
#undef MU
#undef A_PASS1
}

struct WSlice { unsigned w[96]; };

__device__ __forceinline__ void pack_store(const v4u a, const v4u b, int n, WSlice& W, LAS unsigned char* lw) {
  const unsigned av[4] = {a.x, a.y, a.z, a.w}, bv[4] = {b.x, b.y, b.z, b.w};
  unsigned o[8];
#pragma unroll
  for (int i = 0; i < 4; ++i) {
    o[2 * i] = __builtin_amdgcn_perm(bv[i], av[i], 0x05040100u);
    o[2 * i + 1] = __builtin_amdgcn_perm(bv[i], av[i], 0x07060302u);
  }
  if (n < 12) {
#pragma unroll
    for (int e = 0; e < 8; ++e) W.w[8 * n + e] = (unsigned)opaque((int)o[e]);
  } else {
    *(LAS v4u*)(lw + (2 * (n - 12)) * 8192) = v4u{o[0], o[1], o[2], o[3]};
    *(LAS v4u*)(lw + (2 * (n - 12) + 1) * 8192) = v4u{o[4], o[5], o[6], o[7]};
  }
}
template <bool GQA, int DEPTH>
__device__ __forceinline__ void load_wo(const unsigned short* wo, int h0, int w, int lane, WSlice& W, LAS unsigned char* lw) {
  const rsrc_t rs = mkrs(wo, NH * DH * DM * 2);
  const unsigned base = (unsigned)(((h0 + (w >> 2)) * DH + 32 * (w & 3)) * (DM * 2));
  v4u raw[4][8];
#pragma unroll
  for (int st = 0; st < 4 + DEPTH - 1; ++st) {
    if (st < 4) {
      const int g = 3 - st;
#pragma unroll
      for (int pc = 0; pc < 4; ++pc)
#pragma unroll
        for (int e = 0; e < 2; ++e) {
          const int k = GQA ? 8 * (2 * e + (g >> 1)) + 4 * (g & 1) + pc : 8 * g + 2 * pc + e;
          raw[g][2 * pc + e] = ld_nt(rs, lane * 16, base + k * (DM * 2));
        }
      SCHED0();
    }
    if (st >= DEPTH - 1) {
      const int g = 3 - (st - (DEPTH - 1));
#pragma unroll
      for (int pc = 0; pc < 4; ++pc) pack_store(raw[g][2 * pc], raw[g][2 * pc + 1], 4 * g + pc, W, lw);
      SCHED0();
    }
  }
}
template <int DEPTH>
__device__ __forceinline__ void load_wq(const unsigned short* wq, int h0, int w, int lane, WSlice& W, LAS unsigned char* lw) {
  const rsrc_t rs = mkrs(wq, NH * DH * DM * 2);
  const unsigned voff = (unsigned)((lane >> 4) * (NH * DH * 2) + (lane & 15) * 16), base = (unsigned)(64 * w * (NH * DH * 2) + h0 * (DH * 2));
  v4u raw[4][8];
#pragma unroll
  for (int st = 0; st < 4 + DEPTH - 1; ++st) {
    if (st < 4) {
      const int q = 3 - st;
#pragma unroll
      for (int i = 0; i < 4; ++i)
#pragma unroll
        for (int e = 0; e < 2; ++e) {
          const int n = 4 * q + i, n2 = n >> 1, hd = n & 1;
          raw[q][2 * i + e] = ld_nt(rs, voff, base + 4 * (2 * n2 + e) * (NH * DH * 2) + hd * (DH * 2));
        }
      SCHED0();
    }
    if (st >= DEPTH - 1) {
      const int q = 3 - (st - (DEPTH - 1));
#pragma unroll
      for (int i = 0; i < 4; ++i) pack_store(raw[q][2 * i], raw[q][2 * i + 1], 4 * q + i, W, lw);
      SCHED0();
    }
  }
}

struct SvcCtx { rsrc_t rw; LAS unsigned char* L; int h0, j16, w, lane; const unsigned short* x0; unsigned short* out; unsigned char* ws; };

__device__ __forceinline__ void svc_s1(const SvcCtx& c, const WSlice& W, int b, int layer) {
  const int lane = opaque(c.lane), w = c.w, sg = lane >> 2, pc = lane & 3, tid = w * 64 + lane;
  const unsigned rec = rec_base(b, layer, c.h0 + (w >> 2));
  const int nsrc = (layer & 1) ? NSRC_B : NSRC_A;
  const bool v0 = sg < nsrc, v1 = sg + 16 < nsrc;
  const unsigned o0 = sg * REC_B, po = (4 * (w & 3) + pc) * 16;
  typedef unsigned v3u __attribute__((ext_vector_type(3)));
  v4u d[2]; v3u e[2];
  d[0] = v0 ? v4u{SENT, SENT, SENT, SENT} : v4u{0u, 0u, 0u, 0u}; e[0] = v0 ? v3u{SENT, SENT, SENT} : v3u{0xFF800000u, 0u, 0u};
  d[1] = v1 ? v4u{SENT, SENT, SENT, SENT} : v4u{0u, 0u, 0u, 0u}; e[1] = v1 ? v3u{SENT, SENT, SENT} : v3u{0xFF800000u, 0u, 0u};
  for (unsigned spins = 0; spins < SPIN_CAP; ++spins) {
    CFENCE();
    if (__builtin_amdgcn_readfirstlane(__builtin_amdgcn_raw_buffer_load_b32(c.rw, 256 + MK_LIGHTREC * REC_B, rec, 16)) != SENT) break;
  }
  for (unsigned spins = 0; spins < SPIN_CAP; ++spins) {
    CFENCE();
#pragma unroll
    for (int i = 0; i < 2; ++i) {
      if (max(max(e[i].x, e[i].y), e[i].z) == SENT) e[i] = __builtin_amdgcn_raw_buffer_load_b96(c.rw, o0 + i * 16 * REC_B + 256, rec, 16);
      if (umax4(d[i]) == SENT) d[i] = ld_sc1(c.rw, o0 + i * 16 * REC_B + po, rec);
    }
    SCHED0();
    const unsigned mx = max(max(umax4(d[0]), umax4(d[1])), max(max(max(e[0].x, e[0].y), e[0].z), max(max(e[1].x, e[1].y), e[1].z)));
    if (all_lanes(mx != SENT)) break;
  }
  const LAS unsigned char* lw = c.L + S_W + tid * 16;
  v4u xl[4];
#pragma unroll
  for (int i = 0; i < 4; ++i) xl[i] = *(const LAS v4u*)(lw + i * 8192);
  const float m0 = __uint_as_float(e[0].x), m1 = __uint_as_float(e[1].x);
  float M = fmx(m0, m1);
  M = fmx(M, dppf<ROR4>(M)); M = fmx(M, dppf<XOR8>(M)); M = xrow_max(M);
  const float w0 = ex2((m0 - M) * LOG2E), w1 = ex2((m1 - M) * LOG2E);
  float Ls = w0 * __uint_as_float(e[0].y) + w1 * __uint_as_float(e[1].y);
  const float f0 = w0 * __uint_as_float(e[0].z), f1 = w1 * __uint_as_float(e[1].z);
  float o[8];
#pragma unroll
  for (int i = 0; i < 4; ++i) {
    const unsigned u0 = d[0][i], u1 = d[1][i];
    const h16x2 a0 = __builtin_bit_cast(h16x2, u0), a1 = __builtin_bit_cast(h16x2, u1);
    o[2 * i] = (float)a0.x * f0 + (float)a1.x * f1;
    o[2 * i + 1] = (float)a0.y * f0 + (float)a1.y * f1;
  }
  float oa = rs4(o[0], o[1], o[2], o[3]), ob = rs4(o[4], o[5], o[6], o[7]);
  Ls = xrow_sum(Ls);
  oa += dppf<ROR4>(oa); ob += dppf<ROR4>(ob); Ls += dppf<ROR4>(Ls);
  oa += dppf<XOR8>(oa); ob += dppf<XOR8>(ob); Ls += dppf<XOR8>(Ls);
  const float inv = __builtin_amdgcn_rcpf(Ls);
  const unsigned pr = pk2(oa * inv, ob * inv);
  float y[8];
#pragma unroll
  for (int i = 0; i < 8; ++i) y[i] = 0.f;
#pragma unroll
  for (int n = 12; n < 14; ++n) {
    const unsigned op = __builtin_amdgcn_readlane(pr, 16 * (n >> 2) + (n & 3));
#pragma unroll
    for (int i = 0; i < 8; ++i) y[i] = dot2(xl[2 * (n - 12) + (i >> 2)][i & 3], op, y[i]);
  }
  SCHED0();
#pragma unroll
  for (int i = 0; i < 4; ++i) xl[i] = *(const LAS v4u*)(lw + (4 + i) * 8192);
#pragma unroll
  for (int n = 0; n < 12; ++n) {
    const unsigned op = __builtin_amdgcn_readlane(pr, 16 * (n >> 2) + (n & 3));
#pragma unroll
    for (int i = 0; i < 8; ++i) y[i] = dot2(W.w[8 * n + i], op, y[i]);
  }
#pragma unroll
  for (int n = 14; n < 16; ++n) {
    const unsigned op = __builtin_amdgcn_readlane(pr, 16 * (n >> 2) + (n & 3));
#pragma unroll
    for (int i = 0; i < 8; ++i) y[i] = dot2(xl[2 * (n - 14) + (i >> 2)][i & 3], op, y[i]);
  }
  LAS float* red = (LAS float*)(c.L + S_RED);
  *(LAS f32x4*)(red + w * DM + lane * 8) = f32x4{y[0], y[1], y[2], y[3]};
  *(LAS f32x4*)(red + w * DM + lane * 8 + 4) = f32x4{y[4], y[5], y[6], y[7]};
  bar();
  {
    const int g = lane >> 4, cq = lane & 15;
    const LAS float* rp = red + 2 * g * DM + 64 * w + 4 * cq;
    const f32x4 s = *(const LAS f32x4*)(rp) + *(const LAS f32x4*)(rp + DM);
    const float v = rs4(s[0], s[1], s[2], s[3]);
    st_sc1_32(c.rw, (unsigned)((64 * w + 4 * cq + g) * 4), y_base(b, layer) + (unsigned)(c.j16 * (DM * 4)), __float_as_uint(v));
  }
}

__device__ __forceinline__ unsigned x_poll(const SvcCtx& c, unsigned voff, unsigned xo, unsigned first) {
  unsigned v = first;
  for (unsigned spins = 0; !all_lanes(v != 0xFFFFu) && spins < SPIN_CAP; ++spins) {
    CFENCE();
    v = __builtin_amdgcn_raw_buffer_load_b16(c.rw, voff, xo, 16);
  }
  return v;
}

template <bool GATHER>
__device__ __forceinline__ void svc_s2(const SvcCtx& c, const WSlice& W, int b, int layer, unsigned xin = 0u, bool have_x = false) {
  const int lane = opaque(c.lane), w = c.w, g = lane >> 4, cq = lane & 15, tid = w * 64 + lane;
  const int col = 64 * w + 4 * cq + g;
  const unsigned xprev = x_base(b, layer - 1);
  unsigned xb;
  if (have_x) xb = xin;
  else if (!GATHER || layer == 0) xb = c.x0[b * DM + col];
  else xb = __builtin_amdgcn_raw_buffer_load_b16(c.rw, col * 2, xprev, 16);
  float xv;
  if constexpr (GATHER) {
    const unsigned yo = y_base(b, layer);
    const unsigned vo = (unsigned)(4 * g * (DM * 4) + (64 * w + 4 * cq) * 4);
    v4u d[4];
#pragma unroll
    for (int i = 0; i < 4; ++i) d[i] = v4u{SENT, SENT, SENT, SENT};
    if (MK_YDELAY) __builtin_amdgcn_s_sleep(MK_YDELAY);
    for (unsigned spins = 0; spins < SPIN_CAP; ++spins) {
      CFENCE();
#pragma unroll
      for (int i = 0; i < 4; ++i)
        if (umax4(d[i]) == SENT) d[i] = ld_sc1(c.rw, vo + i * (DM * 4), yo);
      SCHED0();
      if (all_lanes(max(max(umax4(d[0]), umax4(d[1])), max(umax4(d[2]), umax4(d[3]))) != SENT)) break;
    }
    if (layer > 0) xb = x_poll(c, col * 2, xprev, xb);
    const f32x4 s = (__builtin_bit_cast(f32x4, d[0]) + __builtin_bit_cast(f32x4, d[1])) + (__builtin_bit_cast(f32x4, d[2]) + __builtin_bit_cast(f32x4, d[3]));
    xb = f2bf(bf2f(xb) + rbf(rs4(s[0], s[1], s[2], s[3])));
    if (c.j16 == 0) {
      __builtin_amdgcn_raw_buffer_store_b16((unsigned short)min(xb, 0xFFFEu), c.rw, col * 2, x_base(b, layer), 16);
    }
  }
  xv = bf2f(xb);
  CFENCE();
  const LAS unsigned char* lw = c.L + S_W + 65536 + tid * 16;
  v4u xl[4];
#pragma unroll
  for (int i = 0; i < 4; ++i) xl[i] = *(const LAS v4u*)(lw + i * 8192);
  const float piv = __builtin_bit_cast(float, __builtin_amdgcn_readfirstlane(__builtin_bit_cast(int, xv)));
  const float dv = xv - piv;
  const float s1 = xrow_sum(row_sum(dv)), s2 = xrow_sum(row_sum(dv * dv));
  LAS float* stt = (LAS float*)(c.L + S_ST);
  if (lane == 0) *(LAS f32x2*)(stt + 2 * w) = f32x2{piv + s1 * (1.f / 64.f), s2 - s1 * s1 * (1.f / 64.f)};
  bar();
  float mean, M2;
  {
    const f32x2 t = *(const LAS f32x2*)(stt + 2 * (lane & 7));
    float ms = t.x;
    ms += dppf<XOR1>(ms); ms += dppf<XOR2>(ms); ms += dppf<XOR7>(ms);
    mean = ms * 0.125f;
    const float dd = t.x - mean;
    M2 = fmaf(64.f * dd, dd, t.y);
    M2 += dppf<XOR1>(M2); M2 += dppf<XOR2>(M2); M2 += dppf<XOR7>(M2);
  }
  const float mb = rbf(mean), den = rbf(__builtin_amdgcn_sqrtf(rbf(rbf(fmx(M2, 0.f) * (1.f / DM)) + 1e-5f)));
  const float xn = rbf(xv - mb) * __builtin_amdgcn_rcpf(den);
  const unsigned pr = pk2(xn, dppf<XOR1>(xn));
  float y[2][8];
#pragma unroll
  for (int i = 0; i < 8; ++i) { y[0][i] = 0.f; y[1][i] = 0.f; }
#define QSTEP_L(N2, BASE)                                                                                   \
  {                                                                                                         \
    const unsigned xp = dppu<BCAST + 2 * (N2)>(pr);                                                         \
    _Pragma("unroll") for (int i = 0; i < 8; ++i) {                                                         \
      y[0][i] = dot2(xl[(i >> 2)][i & 3], xp, y[0][i]);                                                     \
      y[1][i] = dot2(xl[2 + (i >> 2)][i & 3], xp, y[1][i]);                                                 \
    }                                                                                                       \
  }
#define QSTEP_R(N2)                                                                                         \
  {                                                                                                         \
    const unsigned xp = dppu<BCAST + 2 * (N2)>(pr);                                                         \
    _Pragma("unroll") for (int i = 0; i < 8; ++i) {                                                         \
      y[0][i] = dot2(W.w[16 * (N2) + i], xp, y[0][i]);                                                      \
      y[1][i] = dot2(W.w[16 * (N2) + 8 + i], xp, y[1][i]);                                                  \
    }                                                                                                       \
  }
  QSTEP_L(6, 0)
  SCHED0();
#pragma unroll
  for (int i = 0; i < 4; ++i) xl[i] = *(const LAS v4u*)(lw + (4 + i) * 8192);
  QSTEP_R(0) QSTEP_R(1) QSTEP_R(2) QSTEP_R(3) QSTEP_R(4) QSTEP_R(5)
  QSTEP_L(7, 0)
#undef QSTEP_L
#undef QSTEP_R
  LAS float* red = (LAS float*)(c.L + S_RED2);
#pragma unroll
  for (int hd = 0; hd < 2; ++hd) {
    red[w * 256 + hd * DH + cq * 8 + g] = rs4(y[hd][0], y[hd][1], y[hd][2], y[hd][3]);
    red[w * 256 + hd * DH + cq * 8 + 4 + g] = rs4(y[hd][4], y[hd][5], y[hd][6], y[hd][7]);
  }
  bar();
  if (w < 2) {
    f32x2 t[8];
#pragma unroll
    for (int i = 0; i < 8; ++i) t[i] = *(const LAS f32x2*)(red + i * 256 + tid * 2);
    const f32x2 s = ((t[0] + t[1]) + (t[2] + t[3])) + ((t[4] + t[5]) + (t[6] + t[7]));
    st_sc1_32(c.rw, tid * 4, q_base(b, layer + 1) + (unsigned)(c.h0 * (DH * 2)), pk2(s.x, s.y));
  }
}

__device__ __forceinline__ void svc_final(const SvcCtx& c) {
  const int lane = opaque(c.lane), b = c.w, pj = lane >> 3, cq = lane & 7;
  const unsigned yo = y_base(b, 3);
  const unsigned vo = (unsigned)(pj * (DM * 4) + (32 * c.j16 + 4 * cq) * 4);
  const int col = 32 * c.j16 + 4 * cq + (lane >> 4);
  const unsigned xb = x_poll(c, col * 2, x_base(b, 2), 0xFFFFu);
  v4u d0, d1;
  for (unsigned spins = 0;; ++spins) {
    CFENCE();
    d0 = ld_sc1(c.rw, vo, yo); d1 = ld_sc1(c.rw, vo + 8 * (DM * 4), yo);
    if (all_lanes(max(umax4(d0), umax4(d1)) != SENT) || spins > SPIN_CAP) break;
  }
  const f32x4 s = __builtin_bit_cast(f32x4, d0) + __builtin_bit_cast(f32x4, d1);
  float v = rs4(s[0], s[1], s[2], s[3]);
  v += dppf<XOR8>(v);
  if ((lane & 8) == 0) c.out[b * DM + col] = (unsigned short)f2bf(bf2f(xb) + rbf(v));
}

__device__ __forceinline__ void service_role(const Params& p, LAS unsigned char* L, int x, int slot, int w) {
  SvcCtx c;
  c.lane = opaque(lane_id()); c.w = w;
  const int tid = w * 64 + c.lane, set = slot >> 1;
  c.rw = mkrs(p.ws, WS_END); c.L = L; c.h0 = 4 * x + 2 * (slot & 1); c.j16 = 2 * x + (slot & 1); c.x0 = p.x0; c.out = p.out; c.ws = p.ws;
  const unsigned tag = launch_tag(c.rw);
  WSlice Wo, Wq;
  LAS unsigned char* lw = L + S_W + tid * 16;
  if (set == 0) {
    const int col = 64 * w + 4 * (c.lane & 15) + (c.lane >> 4);
    const unsigned xa = p.x0[col], xb = p.x0[DM + col];
    load_wq<4>(p.wqA, c.h0, w, c.lane, Wq, lw + 65536);
    armed_wait(c.rw, tag, 0u, c.lane);
#pragma unroll 1
    for (int b = 0; b < 2; ++b) svc_s2<false>(c, Wq, b, -1, b ? xb : xa, true);
    load_wo<false, 3>(p.woB, c.h0, w, c.lane, Wo, lw);
    armed_wait(c.rw, tag, 1u, c.lane);
  } else {
    load_wo<true, 4>(p.woA, c.h0, w, c.lane, Wo, lw);
    load_wq<3>(p.wqB, c.h0, w, c.lane, Wq, lw + 65536);
    armed_wait(c.rw, tag, 0u, c.lane);
  }
  for (int i = 0; i < 2 * NB; ++i) {
    int b, second;
    if (i < 2 + (set == 0)) { b = i; second = 0; }
    else if (i >= 2 * NB - 2 - (set == 0)) { b = i - NB; second = 1; }
    else if (set == 0) { second = !(i & 1); b = second ? (i - 4) >> 1 : (i + 3) >> 1; }
    else { second = !(i & 1); b = second ? (i - 2) >> 1 : (i + 1) >> 1; }
    const int layer = 2 * second + 1 - set;
    if (set == 1 && i == 2) armed_wait(c.rw, tag, 1u, opaque(c.lane));
    svc_s1(c, Wo, b, layer);
    if (layer < 3) svc_s2<true>(c, Wq, b, layer);
    else bar();
    if (set == 0 && layer == 1 && b + 2 < NB) svc_s2<false>(c, Wq, b + 2, -1);
  }
  if (set == 0) svc_final(c);
  if (blockIdx.x == 0 && tid == 7 * 64) __builtin_amdgcn_raw_buffer_store_b32(tag, c.rw, 0, WS_EPOCH, 16);
  bar();
}

__global__ void __launch_bounds__(NTHR, 2) k_mega(Params p) {
  extern __shared__ __attribute__((aligned(16))) unsigned char smem[];
  LAS unsigned char* L = (LAS unsigned char*)smem;
  const int x = blockIdx.x & 7, slot = blockIdx.x >> 3, w = __builtin_amdgcn_readfirstlane(threadIdx.x >> 6);
  if (slot < 4) service_role(p, L, x, slot, w);
  else stream_role(p, L, x, slot - 4, w);
}
}

extern "C" void kernel_launch(void* const* d_in, const int* in_sizes, int n_in, void* d_out, int out_size, void* d_ws, size_t ws_size, hipStream_t stream) {
  using namespace mk;
  Params p;
  p.x0 = (const unsigned short*)d_in[0];
  p.kA = (const unsigned short*)d_in[1]; p.vA = (const unsigned short*)d_in[2]; p.wqA = (const unsigned short*)d_in[3]; p.woA = (const unsigned short*)d_in[4];
  p.kB = (const unsigned short*)d_in[6]; p.vB = (const unsigned short*)d_in[7]; p.wqB = (const unsigned short*)d_in[8]; p.woB = (const unsigned short*)d_in[9];
  p.out = (unsigned short*)d_out;
  p.ws = (unsigned char*)d_ws;
  (void)hipFuncSetAttribute((const void*)k_mega, hipFuncAttributeMaxDynamicSharedMemorySize, LDS_BYTES);
  k_mega<<<NWG, NTHR, LDS_BYTES, stream>>>(p);
}
```

```cpp
#include <hip/hip_runtime.h>
#include <hip/hip_bf16.h>
#include <stdint.h>

namespace mk {
typedef unsigned v4u __attribute__((ext_vector_type(4)));
typedef unsigned v2u __attribute__((ext_vector_type(2)));
typedef float f32x4 __attribute__((ext_vector_type(4)));
typedef float f32x2 __attribute__((ext_vector_type(2)));
typedef short s16x4 __attribute__((ext_vector_type(4)));
typedef __bf16 bf16x8 __attribute__((ext_vector_type(8)));
typedef __bf16 bf16x2 __attribute__((ext_vector_type(2)));
typedef __amdgpu_buffer_rsrc_t rsrc_t;
#define LAS __attribute__((address_space(3)))

constexpr int NB = 8, DM = 512, NH = 32, HKV = 8, SK = 4096, DH = 128;
constexpr float SCALE = 0.08838834764831845f;
constexpr float LOG2E = 1.4426950408889634f;
constexpr int NWG = 256, NTHR = 512;
constexpr int NSRC_A = 28, NSRC_B = 7;
constexpr int REC_B = 384;
constexpr unsigned SENT = 0xFFFFFFFFu;
constexpr unsigned SPIN_CAP = 1u << 16;
constexpr unsigned O_RA = 0, O_RB = 2 * NH * NSRC_A * REC_B, O_Y = O_RB + 2 * NH * NSRC_B * REC_B, O_Q = O_Y + 4 * 16 * DM * 4, O_X = O_Q + 4 * NH * DH * 2,
                   BLK = O_X + 4 * DM * 2, WS_ARM1 = 2 * BLK, WS_ARM = NB * BLK, WS_FLAG = (WS_ARM + 4095u) / 4096u * 4096u, WS_EPOCH = WS_FLAG + NWG * 64,
                   WS_END = WS_EPOCH + 128;
static_assert(BLK % 128 == 0, "line-aligned blocks");
constexpr int ARM_IT1 = (WS_ARM1 + 224 * 256 * 16 - 1) / (224 * 256 * 16), ARM_IT2 = (WS_ARM - WS_ARM1 + 224 * 256 * 16 - 1) / (224 * 256 * 16);
__device__ __forceinline__ unsigned rec_base(int b, int layer, int head) {
  return (unsigned)b * BLK + ((layer & 1) ? O_RB + (unsigned)(((layer >> 1) * NH + head) * (NSRC_B * REC_B)) : O_RA + (unsigned)(((layer >> 1) * NH + head) * (NSRC_A * REC_B)));
}
__host__ __device__ __forceinline__ unsigned q_base(int b, int layer) { return (unsigned)b * BLK + O_Q + (unsigned)layer * (NH * DH * 2); }
__device__ __forceinline__ unsigned y_base(int b, int layer) { return (unsigned)b * BLK + O_Y + (unsigned)layer * (16 * DM * 4); }
__device__ __forceinline__ unsigned x_base(int b, int layer) { return (unsigned)b * BLK + O_X + (unsigned)layer * (DM * 2); }
constexpr int NS = 14;
constexpr int NPMAX = 19, NO = NPMAX - (NS - 2);
constexpr int W0 = NO - 2, W1 = NO - 1;
constexpr int THR = 5;
constexpr int THR_A = 20, THR_A0 = 8, PF_W = 6;
constexpr int A_BYTES = 147 * 512, BOX = 2080;
constexpr int L_SCR = 2 * A_BYTES, L_QB = L_SCR + 4 * BOX, L_FLG = L_QB + 2 * 1280, L_BB = L_FLG + 256, L_END = L_BB + 3 * 544, LDS_BYTES = 160 * 1024;
constexpr int S_W = 0, S_RED = 2 * 65536, S_RED2 = S_RED + 8 * DM * 4, S_ST = S_RED2 + 8 * 256 * 4;
static_assert(S_ST + 256 <= LDS_BYTES && L_END <= LDS_BYTES, "LDS map");

struct Params {
  const unsigned short *x0, *kA, *vA, *wqA, *woA, *kB, *vB, *wqB, *woB;
  unsigned short* out;
  unsigned char* ws;
};

__device__ __forceinline__ float bf2f(unsigned b) { return __builtin_bit_cast(float, b << 16); }
__device__ __forceinline__ unsigned f2bf(float f) { return (unsigned)__builtin_bit_cast(unsigned short, (__bf16)f); }
__device__ __forceinline__ float rbf(float f) { return bf2f(f2bf(f)); }
__device__ __forceinline__ unsigned pk2(float lo, float hi) { return __builtin_bit_cast(unsigned, bf16x2{(__bf16)lo, (__bf16)hi}); }
__device__ __forceinline__ float dot2(unsigned a, unsigned b, float c) {
  return __builtin_amdgcn_fdot2_f32_bf16(__builtin_bit_cast(bf16x2, a), __builtin_bit_cast(bf16x2, b), c, false);
}
template <int CTRL> __device__ __forceinline__ float dppf(float x) {
  return __builtin_bit_cast(float, __builtin_amdgcn_mov_dpp(__builtin_bit_cast(int, x), CTRL, 0xf, 0xf, true));
}
template <int CTRL> __device__ __forceinline__ unsigned dppu(unsigned x) { return (unsigned)__builtin_amdgcn_mov_dpp((int)x, CTRL, 0xf, 0xf, true); }
constexpr int XOR1 = 0xB1, XOR2 = 0x4E, XOR7 = 0x141, XOR8 = 0x128, ROR4 = 0x124, BCAST = 0x150;
__device__ __forceinline__ float fmx(float a, float b) { return __builtin_fmaxf(a, b); }
__device__ __forceinline__ float xrow_max(float x) {
  auto s = __builtin_amdgcn_permlane16_swap(__float_as_uint(x), __float_as_uint(x), false, false);
  x = fmx(__uint_as_float(s[0]), __uint_as_float(s[1]));
  auto t = __builtin_amdgcn_permlane32_swap(__float_as_uint(x), __float_as_uint(x), false, false);
  return fmx(__uint_as_float(t[0]), __uint_as_float(t[1]));
}
__device__ __forceinline__ float xrow_sum(float x) {
  auto s = __builtin_amdgcn_permlane16_swap(__float_as_uint(x), __float_as_uint(x), false, false);
  x = __uint_as_float(s[0]) + __uint_as_float(s[1]);
  auto t = __builtin_amdgcn_permlane32_swap(__float_as_uint(x), __float_as_uint(x), false, false);
  return __uint_as_float(t[0]) + __uint_as_float(t[1]);
}
__device__ __forceinline__ float rs4(float a, float b, float c, float d) {
  auto s = __builtin_amdgcn_permlane16_swap(__float_as_uint(a), __float_as_uint(b), false, false);
  auto t = __builtin_amdgcn_permlane16_swap(__float_as_uint(c), __float_as_uint(d), false, false);
  const float u = __uint_as_float(s[0]) + __uint_as_float(s[1]), v = __uint_as_float(t[0]) + __uint_as_float(t[1]);
  auto r = __builtin_amdgcn_permlane32_swap(__float_as_uint(u), __float_as_uint(v), false, false);
  return __uint_as_float(r[0]) + __uint_as_float(r[1]);
}
__device__ __forceinline__ float row_max(float x) {
  x = fmx(x, dppf<XOR1>(x)); x = fmx(x, dppf<XOR2>(x)); x = fmx(x, dppf<ROR4>(x)); return fmx(x, dppf<XOR8>(x));
}
__device__ __forceinline__ float row_sum(float x) {
  x += dppf<XOR1>(x); x += dppf<XOR2>(x); x += dppf<ROR4>(x); return x + dppf<XOR8>(x);
}
__device__ __forceinline__ float ex2(float x) { return __builtin_amdgcn_exp2f(x); }
__device__ __forceinline__ rsrc_t mkrs(const void* p, unsigned bytes) { return __builtin_amdgcn_make_buffer_rsrc((void*)p, 0, (int)bytes, 0x00020000); }
__device__ __forceinline__ v4u ld_nt(rsrc_t rs, unsigned voff, unsigned soff) { return __builtin_amdgcn_raw_buffer_load_b128(rs, voff, soff, 2); }
__device__ __forceinline__ v4u ld_sc1(rsrc_t rs, unsigned voff, unsigned soff) { return __builtin_amdgcn_raw_buffer_load_b128(rs, voff, soff, 16); }
__device__ __forceinline__ v2u ld_sc1_64(rsrc_t rs, unsigned voff, unsigned soff) { return __builtin_amdgcn_raw_buffer_load_b64(rs, voff, soff, 16); }
__device__ __forceinline__ unsigned nosent(unsigned x) { return x < 0xFFFFFFFEu ? x : 0xFFFFFFFEu; }
__device__ __forceinline__ void st_sc1(rsrc_t rs, unsigned voff, unsigned soff, v4u v) {
  v.x = nosent(v.x); v.y = nosent(v.y); v.z = nosent(v.z); v.w = nosent(v.w);
  __builtin_amdgcn_raw_buffer_store_b128(v, rs, voff + soff, 0, 16);
}
__device__ __forceinline__ void st_sc1_64(rsrc_t rs, unsigned voff, unsigned soff, v2u v) {
  v.x = nosent(v.x); v.y = nosent(v.y);
  __builtin_amdgcn_raw_buffer_store_b64(v, rs, voff, soff, 16);
}
__device__ __forceinline__ void st_sc1_32(rsrc_t rs, unsigned voff, unsigned soff, unsigned v) { __builtin_amdgcn_raw_buffer_store_b32(nosent(v), rs, voff, soff, 16); }
__device__ __forceinline__ unsigned umax4(v4u v) { return max(max(v.x, v.y), max(v.z, v.w)); }
__device__ __forceinline__ bool all_lanes(bool ok) { return __builtin_amdgcn_ballot_w64(!ok) == 0ull; }
#define CFENCE() asm volatile("" ::: "memory")
__device__ __forceinline__ int lane_id() { return (int)__builtin_amdgcn_mbcnt_hi(~0u, __builtin_amdgcn_mbcnt_lo(~0u, 0u)); }
__device__ __forceinline__ int opaque(int v) { asm volatile("" : "+v"(v)); return v; }
#define SCHED0() __builtin_amdgcn_sched_barrier(0)
template <int N> __device__ __forceinline__ void wait_vm() { asm volatile("s_waitcnt vmcnt(%0)" ::"n"(N) : "memory"); }
__device__ __forceinline__ void bar() { asm volatile("s_waitcnt lgkmcnt(0)\n\ts_barrier" ::: "memory"); }
__device__ __forceinline__ void wait_lgkm() { asm volatile("s_waitcnt lgkmcnt(0)" ::: "memory"); }
__device__ __forceinline__ f32x4 mfma16(unsigned a0, unsigned a1, unsigned b0, unsigned b1, f32x4 c) {
  return __builtin_amdgcn_mfma_f32_16x16x16bf16_1k(__builtin_bit_cast(s16x4, v2u{a0, a1}), __builtin_bit_cast(s16x4, v2u{b0, b1}), c, 0, 0, 0);
}
__device__ __forceinline__ f32x4 mfma32(v4u a, v4u b, f32x4 c) {
  return __builtin_amdgcn_mfma_f32_16x16x32_bf16(__builtin_bit_cast(bf16x8, a), __builtin_bit_cast(bf16x8, b), c, 0, 0, 0);
}
typedef _Float16 h16x2 __attribute__((ext_vector_type(2)));
__device__ __forceinline__ unsigned pkh(float lo, float hi) { return __builtin_bit_cast(unsigned, h16x2{(_Float16)lo, (_Float16)hi}); }
__device__ __forceinline__ f32x2 rec_scale(float amax) {
  unsigned e = __float_as_uint(amax) & 0x7F800000u;
  e = min(max(e, 0x01000000u), 0x7E000000u);
  return f32x2{__uint_as_float(e), __uint_as_float(0x7F000000u - e)};
}
__device__ __forceinline__ unsigned launch_tag(rsrc_t rw) {
  return __builtin_amdgcn_readfirstlane(__builtin_amdgcn_raw_buffer_load_b32(rw, 0, WS_EPOCH, 16)) * 2654435761u + 12345u;
}
template <int ITERS, unsigned LO, unsigned HI>
__device__ __forceinline__ void arm_stores(rsrc_t rw, int sidx, int t256) {
  const unsigned sv = (unsigned)opaque(-1);
#pragma unroll
  for (int i = 0; i < ITERS; ++i) {
    const unsigned off = LO + ((unsigned)(sidx * ITERS + i) * 256u + (unsigned)t256) * 16u;
    if (off < HI) __builtin_amdgcn_raw_buffer_store_b128(v4u{sv, sv, sv, sv}, rw, off, 0, 16);
  }
}
__device__ __forceinline__ void armed_wait(rsrc_t rw, unsigned tag, unsigned stage, int lane) {
  for (unsigned spins = 0; spins < SPIN_CAP; ++spins) {
    CFENCE();
    bool ok = true;
#pragma unroll
    for (int i = 0; i < 4; ++i) {
      const unsigned d = __builtin_amdgcn_raw_buffer_load_b32(rw, (unsigned)min(i * 64 + lane, 223) * 64u, WS_FLAG, 16) - tag;
      ok &= d >= stage && d <= 1u;
    }
    if (all_lanes(ok)) break;
    __builtin_amdgcn_s_sleep(4);
  }
  CFENCE();
}
__device__ __forceinline__ void lds_wait_ge(volatile LAS unsigned* f, unsigned v) {
  for (unsigned spins = 0; *f < v && spins < (SPIN_CAP << 7); ++spins) __builtin_amdgcn_s_sleep(1);
  CFENCE();
}
__device__ __forceinline__ void lds_wait_ge_sleep(volatile LAS unsigned* f, unsigned v) {
  for (unsigned spins = 0; *f < v && spins < (SPIN_CAP << 6); ++spins) __builtin_amdgcn_s_sleep(2);
  CFENCE();
}
__device__ __forceinline__ void lds_wait_ge4(volatile LAS v4u* f, unsigned v) {
  for (unsigned spins = 0; spins < (SPIN_CAP << 8); ++spins) {
    const v4u t = *f;
    if (min(min(t.x, t.y), min(t.z, t.w)) >= v) break;
  }
  CFENCE();
}

struct SoftState { float m, l; f32x4 a; };

template <int U0, int NU>
__device__ __forceinline__ float b_scores(const v4u (&K)[2 * NS], const v4u q, int r) {
  constexpr int P2 = NU > 8 ? 16 : NU > 4 ? 8 : NU > 2 ? 4 : 2;
  float t[16];
#pragma unroll
  for (int u = 0; u < NU; ++u) {
    const v4u k = K[(U0 + u) % (2 * NS)];
    t[u] = dot2(k.w, q.w, dot2(k.z, q.z, dot2(k.y, q.y, dot2(k.x, q.x, 0.f))));
  }
#define TR_STEP(HALF, CTRL, CNT)                                                                  \
  _Pragma("unroll") for (int i = 0; i < (HALF); ++i) {                                            \
    if (i < (CNT)) {                                                                              \
      const float a_ = t[i] + dppf<CTRL>(t[i]);                                                   \
      if (i + (HALF) < (CNT)) { const float b_ = t[i + (HALF)] + dppf<CTRL>(t[i + (HALF)]); t[i] = (r & (HALF)) ? b_ : a_; } \
      else t[i] = a_;                                                                             \
    }                                                                                             \
  }
  if constexpr (P2 >= 16) { TR_STEP(8, XOR8, NU) }
  if constexpr (P2 >= 8) { TR_STEP(4, XOR7, (NU < 8 ? NU : 8)) }
  if constexpr (P2 >= 4) { TR_STEP(2, XOR2, (NU < 4 ? NU : 4)) }
  TR_STEP(1, XOR1, 2)
#undef TR_STEP
  float x = t[0];
  if constexpr (P2 < 4) x += dppf<XOR2>(x);
  if constexpr (P2 < 8) x += dppf<ROR4>(x);
  if constexpr (P2 < 16) x += dppf<XOR8>(x);
  return x;
}
template <int U0, int NU>
__device__ __forceinline__ f32x4 b_pv(const v4u (&V)[2 * NS], unsigned pp, const v4u mk, f32x4 a) {
#define PV(U)                                                                                     \
  if constexpr ((U) < NU) {                                                                       \
    const unsigned bc = dppu<BCAST + (U)>(pp);                                                    \
    a = mfma32(V[(U0 + (U)) % (2 * NS)], v4u{bc & mk.x, bc & mk.y, bc & mk.z, bc & mk.w}, a);     \
  }
  PV(0) PV(1) PV(2) PV(3) PV(4) PV(5) PV(6) PV(7) PV(8) PV(9) PV(10) PV(11) PV(12) PV(13) PV(14) PV(15)
#undef PV
  return a;
}
template <int U0, int NU, bool FIRST>
__device__ __forceinline__ void b_chunk(const v4u (&K)[2 * NS], const v4u (&V)[2 * NS], const v4u q, bool ok0, SoftState& st, int r, const v4u mk) {
  constexpr int N0 = NU > 16 ? 16 : NU, N1 = NU - N0;
  const float x0 = b_scores<U0, N0>(K, q, r);
  const float s0 = (r < N0 && ok0) ? rbf(rbf(x0) * SCALE) : -INFINITY;
  float s1 = -INFINITY;
  if constexpr (N1 > 0) {
    const float x1 = b_scores<U0 + 16, N1>(K, q, r);
    s1 = r < N1 ? rbf(rbf(x1) * SCALE) : -INFINITY;
  }
  float mn = xrow_max(row_max(fmx(s0, s1)));
  if constexpr (!FIRST) mn = fmx(mn, st.m);
  const float p0 = ex2((s0 - mn) * LOG2E);
  if constexpr (FIRST) {
    st.l = p0;
    st.a = f32x4{0.f, 0.f, 0.f, 0.f};
  } else {
    const float alpha = ex2((st.m - mn) * LOG2E);
    st.l = st.l * alpha + p0;
    st.a *= alpha;
  }
  st.m = mn;
  st.a = b_pv<U0, N0>(V, pk2(p0, p0), mk, st.a);
  if constexpr (N1 > 0) {
    const float p1 = ex2((s1 - mn) * LOG2E);
    st.l += p1;
    st.a = b_pv<U0 + 16, N1>(V, pk2(p1, p1), mk, st.a);
  }
  asm volatile("" : "+v"(st.l));
}
__device__ __forceinline__ v4u pv_masks(int lane) {
  const unsigned h = (lane & 1) ? 0xFFFF0000u : 0xFFFFu;
  const int t = (lane >> 1) & 3;
  return v4u{t == 0 ? h : 0u, t == 1 ? h : 0u, t == 2 ? h : 0u, t == 3 ? h : 0u};
}

template <int SLOT, int TH>
__device__ __forceinline__ void issue_pair(v4u (&K)[2 * NS], v4u (&V)[2 * NS], rsrc_t rk, rsrc_t rv, unsigned va, unsigned vb, unsigned soff) {
  wait_vm<TH>();
  K[2 * SLOT] = ld_nt(rk, va, soff);
  SCHED0();
  wait_vm<TH>();
  K[2 * SLOT + 1] = ld_nt(rk, vb + 1024, soff);
  SCHED0();
  wait_vm<TH>();
  V[2 * SLOT] = ld_nt(rv, va, soff);
  SCHED0();
  wait_vm<TH>();
  V[2 * SLOT + 1] = ld_nt(rv, vb + 1024, soff);
  SCHED0();
}

struct StreamCtx {
  rsrc_t rw;
  v4u rwv;
  LAS unsigned char* L;
  int lane, grp, wq, x, head, c28;
  int nk;
  unsigned ldsA;
  unsigned a_off, a_tail;
};
__device__ __forceinline__ volatile LAS unsigned* flags(const StreamCtx& c) { return (volatile LAS unsigned*)(c.L + L_FLG); }
__device__ __forceinline__ unsigned a_region(int grp, int c28, int wq) {
  return (unsigned)(grp * A_BYTES + (((c28 * 4 + wq) * 4096) / 112 - (c28 * 16384) / 112) * 512);
}

template <int TH>
__device__ __forceinline__ void a_issue(const Params& p, const StreamCtx& c, int b, unsigned region) {
  const int lane = opaque(c.lane), a = lane >> 4, pos = lane & 15;
  const rsrc_t rk = mkrs(p.kA, NB * HKV * SK * DH * 2), rv = mkrs(p.vA, NB * HKV * SK * DH * 2);
  const unsigned so = (unsigned)b * (HKV * SK * DH * 2) + c.a_off;
  const unsigned kv = a * 256 + ((pos ^ (4 * a)) * 16), vv = lane * 16;
  LAS unsigned char* kd = c.L + region;
  LAS unsigned char* vd = kd + c.nk * 256;
  if (c.nk == 37 && lane < 16) {
    __builtin_amdgcn_raw_ptr_buffer_load_lds(rk, (LAS void*)(kd + 9 * 1024), 16, kv ^ 16, so + c.a_tail, 0, 2);
    __builtin_amdgcn_raw_ptr_buffer_load_lds(rv, (LAS void*)(vd + 9 * 1024), 16, vv, so + c.a_tail, 0, 2);
  }
#pragma unroll
  for (int u = 0; u < 9; ++u) {
    wait_vm<TH>();
    __builtin_amdgcn_raw_ptr_buffer_load_lds(rk, (LAS void*)(kd + u * 1024), 16, kv ^ ((u & 3) * 16), so + u * 4096, 0, 2);
    __builtin_amdgcn_raw_ptr_buffer_load_lds(rv, (LAS void*)(vd + u * 1024), 16, vv, so + u * 4096, 0, 2);
  }
}

__device__ __forceinline__ void b_publish(const StreamCtx& c, int b, int layer, const SoftState& st, unsigned seq, unsigned bbase = L_BB, int f0 = 44, int bstr = 544) {
  const int lane = opaque(c.lane), cc = lane & 15, lg = lane >> 4;
  volatile LAS unsigned* fl = flags(c) + (f0 - 44);
  float l = xrow_sum(row_sum(st.l)), m = st.m;
  const unsigned pos = (unsigned)(lg * 8 + (cc & 7)) * 16u;
  if (c.wq < 3) {
    LAS unsigned char* o = c.L + bbase + c.wq * bstr;
    lds_wait_ge(fl + 47, seq - 1);
    if (cc < 8) *(LAS f32x4*)(o + pos) = st.a;
    if (lane == 0) *(LAS f32x2*)(o + 512) = f32x2{m, l};
    wait_lgkm();
    if (lane == 0) fl[44 + c.wq] = seq;
  } else {
    for (unsigned spins = 0; spins < (SPIN_CAP << 8); ++spins) {
      const v4u t = *(volatile LAS v4u*)(fl + 44);
      if (min(min(t.x, t.y), t.z) >= seq) break;
    }
    CFENCE();
    f32x4 a[3]; f32x2 ml[3];
#pragma unroll
    for (int i = 0; i < 3; ++i) { a[i] = *(const LAS f32x4*)(c.L + bbase + i * bstr + pos); ml[i] = *(const LAS f32x2*)(c.L + bbase + i * bstr + 512); }
    const float M = fmx(fmx(m, ml[0].x), fmx(ml[1].x, ml[2].x));
    const float e = ex2((m - M) * LOG2E);
    f32x4 o = st.a * e;
    l *= e;
#pragma unroll
    for (int i = 0; i < 3; ++i) { const float ei = ex2((ml[i].x - M) * LOG2E); o += a[i] * ei; l = fmaf(ml[i].y, ei, l); }
    CFENCE();
    if (lane == 0) fl[47] = seq;
    const f32x2 sc = rec_scale(xrow_max(row_max(fmx(fmx(fabsf(o[0]), fabsf(o[1])), fmx(fabsf(o[2]), fabsf(o[3]))))));
    o *= sc.y;
    const unsigned ro = rec_base(b, layer, c.head) + (unsigned)((c.c28 % 7) * REC_B);
    if (cc < 8) st_sc1_64(c.rw, pos >> 1, ro, v2u{pkh(o[0], o[1]), pkh(o[2], o[3])});
    if (lane == 8) st_sc1(c.rw, 256, ro, v4u{__float_as_uint(M), __float_as_uint(l), __float_as_uint(sc.x), 0u});
  }
  CFENCE();
}

template <bool SHARED>
__device__ __forceinline__ void a_merge(const StreamCtx& c, int b, int layer) {
  const int lane = opaque(c.lane), kg = lane >> 4, col = lane & 15, wq = c.wq;
  unsigned src;
  if constexpr (SHARED) src = (unsigned)(L_SCR + kg * BOX);
  else {
    const unsigned r0 = a_region(c.grp, c.c28, 0), r1 = a_region(c.grp, c.c28, 1), r2 = a_region(c.grp, c.c28, 2), r3 = a_region(c.grp, c.c28, 3);
    src = (kg & 2) ? ((kg & 1) ? r3 : r2) : ((kg & 1) ? r1 : r0);
  }
  const LAS unsigned char* in = c.L + src;
  const f32x2 ml = *(const LAS f32x2*)(in + 2048 + wq * 8);
  f32x4 b0 = *(const LAS f32x4*)(in + (wq * 16 + col) * 32), b1 = *(const LAS f32x4*)(in + (wq * 16 + col) * 32 + 16);
  const float M = xrow_max(ml.x), e = ex2((ml.x - M) * LOG2E);
  b0 *= e; b1 *= e;
  const float Lt = xrow_sum(ml.y * e);
  const float o0 = rs4(b0[0], b0[1], b0[2], b0[3]), o1 = rs4(b1[0], b1[1], b1[2], b1[3]);
  const f32x2 sc = rec_scale(xrow_max(row_max(fmx(fabsf(o0), fabsf(o1)))));
  const unsigned ro = rec_base(b, layer, 4 * c.x + wq) + (unsigned)(c.c28 * REC_B);
  st_sc1_32(c.rw, (unsigned)(col * 16 + kg * 4), ro, pkh(o0 * sc.y, o1 * sc.y));
  if (lane == 0) st_sc1(c.rw, 256, ro, v4u{__float_as_uint(M), __float_as_uint(Lt), __float_as_uint(sc.x), 0u});
  CFENCE();
}
template <int NBYTES>
__device__ __forceinline__ void q_dma(const StreamCtx& c, unsigned qoff) {
  const unsigned dst = (unsigned)(size_t)(c.L + L_QB + c.grp * 1280 + (NBYTES == 1024 ? 0 : 1024));
  const unsigned voff = opaque(c.lane) * 16;
  if (voff < NBYTES) {
    unsigned keep;
    asm volatile("s_mov_b32 %0, m0\n\ts_mov_b32 m0, %1\n\ts_nop 4\n\tbuffer_load_dwordx4 %2, %3, %4 offen sc1 lds\n\ts_mov_b32 m0, %0"
                 : "=&s"(keep) : "s"(dst), "v"(voff), "s"(c.rwv), "s"(qoff) : "memory");
  }
}
template <int NBYTES>
__device__ __forceinline__ void q_arm(const StreamCtx& c, unsigned qoff, unsigned seq) {
  if (c.wq == 0) {
    const int lane = opaque(c.lane);
    const unsigned sv = (unsigned)opaque(-1);
    *(LAS v4u*)(c.L + L_QB + c.grp * 1280 + (NBYTES == 1024 ? 0 : 1024) + ((lane * 16) & (NBYTES - 1))) = v4u{sv, sv, sv, sv};
    wait_lgkm();
    if (lane == 0) flags(c)[12 + c.grp] = seq;
    q_dma<NBYTES>(c, qoff);
  }
}
template <int NBYTES>
__device__ __forceinline__ void q_wait(const StreamCtx& c, unsigned qoff, unsigned seq) {
  const int lane = opaque(c.lane);
  volatile LAS v4u* qp = (volatile LAS v4u*)(c.L + L_QB + c.grp * 1280 + (NBYTES == 1024 ? 0 : 1024) + ((lane * 16) & (NBYTES - 1)));
  volatile LAS unsigned* armed = flags(c) + 12 + c.grp;
  for (unsigned spins = 0; spins < (SPIN_CAP << 6); ++spins) {
    const unsigned a = *armed;
    const v4u q = *qp;
    if (all_lanes(a >= seq && umax4(q) != SENT)) break;
    if (c.wq == 0 && (spins & 1) == 1) q_dma<NBYTES>(c, qoff);
    __builtin_amdgcn_s_sleep(1);
  }
  CFENCE();
}

struct AScores { float s[10]; };
__device__ __forceinline__ AScores a_qk(const StreamCtx& c, unsigned region) {
  const int lane = opaque(c.lane), kg = lane >> 4, col = lane & 15, hh = col >> 2;
  const LAS unsigned char* base = c.L + region;
  const LAS unsigned char* qb = c.L + L_QB + c.grp * 1280 + hh * 256 + kg * 16;
  f32x4 sc[3] = {{0.f, 0.f, 0.f, 0.f}, {0.f, 0.f, 0.f, 0.f}, {0.f, 0.f, 0.f, 0.f}};
  const unsigned ro = (unsigned)((col & 3) * 1024 + (col >> 2) * 256);
#pragma unroll
  for (int th = 0; th < 2; ++th) {
    v4u qf[2], kf[2][3];
#pragma unroll
    for (int t2 = 0; t2 < 2; ++t2) {
      const int t = 2 * th + t2;
      const LAS unsigned char* kp = base + ro + 16 * ((4 * t + kg) ^ col);
      qf[t2] = *(const LAS v4u*)(qb + t * 64);
#pragma unroll
      for (int tau = 0; tau < 3; ++tau) kf[t2][tau] = *(const LAS v4u*)(kp + tau * 4096);
    }
    SCHED0();
#pragma unroll
    for (int t2 = 0; t2 < 2; ++t2)
#pragma unroll
      for (int tau = 0; tau < 3; ++tau) sc[tau] = mfma32(kf[t2][tau], qf[t2], sc[tau]);
  }
  AScores o;
#pragma unroll
  for (int u = 0; u < 10; ++u) o.s[u] = rbf(rbf(sc[u >> 2][u & 3]) * SCALE);
  if (!(c.nk == 37 && kg == 0)) o.s[9] = -INFINITY;
  return o;
}
__device__ __forceinline__ void a_pv(const StreamCtx& c, const AScores& sc, unsigned region, unsigned box) {
  const int lane = opaque(c.lane), kg = lane >> 4, col = lane & 15, hh = col >> 2;
  const unsigned m0 = (lane & 3) == 0 ? 0xFFFFu : (lane & 3) == 1 ? 0xFFFF0000u : 0u;
  const unsigned m1 = (lane & 3) == 2 ? 0xFFFFu : (lane & 3) == 3 ? 0xFFFF0000u : 0u;
  const LAS unsigned char* vbase = c.L + region + c.nk * 256 + lane * 16;
  float mx = sc.s[0];
#pragma unroll
  for (int u = 1; u < 10; ++u) mx = fmx(mx, sc.s[u]);
  const float m = xrow_max(mx), mneg = -m * LOG2E;
  float l = 0.f;
  f32x4 a0 = {0.f, 0.f, 0.f, 0.f}, a1 = {0.f, 0.f, 0.f, 0.f};
#pragma unroll
  for (int u = 0; u < 10; ++u) {
    if (u < 9 || c.nk == 37) {
      const float pr = ex2(fmaf(sc.s[u], LOG2E, mneg));
      l += pr;
      const unsigned pp = pk2(pr, pr), b0 = pp & m0, b1 = pp & m1;
      v4u v = *(const LAS v4u*)(vbase + u * 1024);
      if (u == 9 && kg != 0) v = v4u{0u, 0u, 0u, 0u};
      a0 = mfma16(v.x, v.y, b0, b1, a0);
      a1 = mfma16(v.z, v.w, b0, b1, a1);
    }
  }
  l = xrow_sum(l);
  LAS unsigned char* o = c.L + box;
  const unsigned id = (unsigned)(hh * 16 + kg * 4 + (col & 3));
  *(LAS f32x4*)(o + id * 32) = a0;
  *(LAS f32x4*)(o + id * 32 + 16) = a1;
  if ((lane & 0x33) == 0) *(LAS f32x2*)(o + 2048 + hh * 8) = f32x2{m, l};
  wait_lgkm();
}

__device__ __forceinline__ void stream_role(const Params& p, LAS unsigned char* L, int x, int c28, int w) {
  StreamCtx c;
  const int tid = w * 64 + opaque(lane_id());
  c.rw = mkrs(p.ws, WS_END);
  c.rwv = v4u{(unsigned)(size_t)p.ws, (unsigned)((size_t)p.ws >> 32) & 0xFFFFu, WS_END, 0x00020000u};
  c.L = L; c.lane = tid & 63; c.grp = w >> 2; c.wq = w & 3; c.x = x; c.c28 = c28;
  c.head = 4 * x + c28 / 7;
  const int a112 = c28 * 4 + c.wq, ka0 = (a112 * 4096) / 112;
  c.nk = ((a112 + 1) * 4096) / 112 - ka0;
  c.ldsA = a_region(c.grp, c28, c.wq);
  const int ka_cu = (c28 * 16384) / 112;
  c.a_off = (unsigned)((x * SK + ka_cu) * (DH * 2)) + (unsigned)c.wq * 1024u;
  c.a_tail = (unsigned)((144 + (ka0 - ka_cu) - 36 * c.wq) * (DH * 2)) - (unsigned)c.wq * 1024u;
  const int c7 = c28 % 7, kb0 = (c7 * 4096) / 7, ntail = ((c7 + 1) * 4096) / 7 - kb0 - 576;
  const int t0 = (c.wq * ntail) / 4, rem = ((c.wq + 1) * ntail) / 4 - t0;
  const unsigned b_off = (unsigned)((c.head * SK + kb0) * (DH * 2)) + (unsigned)c.wq * 2048u;
  const unsigned p0_off = (unsigned)((576 + t0) * (DH * 2)) - (unsigned)c.wq * 2048u;
  constexpr unsigned BATCH_B = NH * SK * DH * 2, OOB = 0x40000000u;
  const rsrc_t rk = mkrs(p.kB, NB * BATCH_B), rv = mkrs(p.vB, NB * BATCH_B);
  const unsigned qa_off = (unsigned)(4 * x * DH * 2), qb_off = (unsigned)(c.head * DH * 2);
  volatile LAS unsigned* fl = flags(c);
  if (tid < 64) ((LAS unsigned*)(L + L_FLG))[tid] = 0u;
  bar();

  v4u K[2 * NS], V[2 * NS];
  SoftState st4 = {-INFINITY, 0.f, {0.f, 0.f, 0.f, 0.f}};
  v4u q4 = {0u, 0u, 0u, 0u};
  unsigned qseq = 1;

#define POFF(i) ((i) == 0 ? p0_off : (unsigned)((i)-1) * 8192u)
#define RRS(i, slot) issue_pair<(slot), THR>(K, V, rk, rv, (i) == 0 ? voff0 : voff, (i) == 0 ? OOB : voff, sp + POFF(i))
#define MAIN(i) issue_pair<(i) % NS, THR>(K, V, rk, rv, (i) == 0 ? voff0 : voff, (i) == 0 ? OOB : voff, so + POFF(i))
#define MU(i) (2 * ((i) % NS))
#define LANE_CONSTS                                                                  \
  const int lane = opaque(c.lane), r = lane & 15;                                    \
  const v4u mk = pv_masks(lane);                                                     \
  const unsigned voff = lane * 16, voff0 = voff | ((lane >> 4) >= rem ? OOB : 0u);   \
  const bool ok0 = r >= 2 || (r == 0 && (lane >> 4) < rem);
#define RR_STEP(i, last)                                                             \
  if constexpr ((i) <= (last)) { b_chunk<2 * W0, 2, false>(K, V, q4, (i) == 0 ? ok0 : true, st4, r, mk); SCHED0(); } \
  if constexpr ((i) + 2 <= (last)) { RRS((i) + 2, W0); }                             \
  if constexpr ((i) + 1 <= (last)) { b_chunk<2 * W1, 2, false>(K, V, q4, true, st4, r, mk); SCHED0(); } \
  if constexpr ((i) + 3 <= (last)) { RRS((i) + 3, W1); }
#define A_PASS1(bb, sq_, reg_)                                                      \
  {                                                                                 \
    q_wait<1024>(c, qa_off + q_base(bb, 0), sq_);                                   \
    const AScores sc = a_qk(c, reg_);                                               \
    lds_wait_ge4((volatile LAS v4u*)(fl + 4), (unsigned)(bb));                      \
    if ((bb) > 4) lds_wait_ge(fl + 51, (unsigned)((bb)-4));                         \
    a_pv(c, sc, reg_, (unsigned)(L_SCR + c.wq * BOX));                              \
    if (c.lane == 0) fl[8 + c.wq] = (unsigned)((bb) + 1);                           \
    lds_wait_ge4((volatile LAS v4u*)(fl + 8), (unsigned)((bb) + 1));                \
    a_merge<true>(c, bb, 0);                                                        \
    if (c.lane == 0) fl[4 + c.wq] = (unsigned)((bb) + 1);                           \
    SCHED0();                                                                       \
  }
  static_assert(NS == 14 && NO == 7, "static schedule below");

  if (c.grp == 1) {
    const unsigned tag = launch_tag(c.rw);
#pragma unroll 1
    for (int bb = 0; bb < 2; ++bb) {
      a_issue<THR_A0>(p, c, bb, c.ldsA - (1 - bb) * A_BYTES);
      wait_vm<0>();
      if (bb == 0 && c.wq == 0) armed_wait(c.rw, tag, 0u, opaque(c.lane));
      const unsigned sq = qseq++;
      q_arm<1024>(c, qa_off + q_base(bb, 0), sq);
      A_PASS1(bb, sq, c.ldsA - (1 - bb) * A_BYTES)
    }
    arm_stores<ARM_IT2, WS_ARM1, WS_ARM>(c.rw, c28 * 8 + x, c.wq * 64 + opaque(c.lane));
    wait_vm<0>();
    if (c.lane == 0) __hip_atomic_fetch_add((LAS unsigned*)(L + L_FLG) + 57, 1u, __ATOMIC_RELAXED, __HIP_MEMORY_SCOPE_WORKGROUP);
    if (c.wq == 3) {
      lds_wait_ge(fl + 57, 4u);
      if (c.lane == 0) __builtin_amdgcn_raw_buffer_store_b32(tag + 1u, c.rw, (unsigned)(c28 * 8 + x) * 64u, WS_FLAG, 16);
    }
    if (c.wq == 0) {
      armed_wait(c.rw, tag, 1u, opaque(c.lane));
      if (c.lane == 0) fl[58] = 1u;
    }
  } else {
    const int lane = opaque(c.lane);
    const unsigned e0 = __builtin_amdgcn_raw_buffer_load_b32(c.rw, 0, WS_EPOCH, 16);
    arm_stores<ARM_IT1, 0u, WS_ARM1>(c.rw, c28 * 8 + x, c.wq * 64 + lane);
#pragma unroll
    for (int i = 0; i < 19; ++i) {
      const int ch = min(i * 112 + a112, 2047), m = ch >> 9, row = ch & 511;
      const unsigned short* src = (m & 1) ? (m == 1 ? p.woA : p.woB) + (size_t)(512 * x + row) * DM : (m == 0 ? p.wqA : p.wqB) + (size_t)row * (NH * DH) + 512 * x;
      if (i >= PF_W) wait_vm<PF_W>();
      if (i == PF_W) {
        if (lane == 0) __hip_atomic_fetch_add((LAS unsigned*)(L + L_FLG) + 56, 1u, __ATOMIC_RELAXED, __HIP_MEMORY_SCOPE_WORKGROUP);
        if (c.wq == 3) {
          lds_wait_ge(fl + 56, 4u);
          if (lane == 0) __builtin_amdgcn_raw_buffer_store_b32(e0 * 2654435761u + 12345u, c.rw, (unsigned)(c28 * 8 + x) * 64u, WS_FLAG, 16);
        }
      }
      K[i] = __builtin_nontemporal_load((const v4u*)(src + lane * 8));
    }
#pragma unroll
    for (int i = 0; i < 19; ++i) asm volatile("" ::"v"(K[i]));
    if (c.wq == 0) armed_wait(c.rw, __builtin_amdgcn_readfirstlane(e0) * 2654435761u + 12345u, 0u, lane);
  }

  for (int it = 0; it < 4; ++it) {
    const int b = 2 * it + c.grp;
    const unsigned so = (unsigned)b * BATCH_B + b_off;
    {
      lds_wait_ge_sleep(fl + c.wq, (unsigned)b);
      LANE_CONSTS
      MAIN(0); MAIN(1); MAIN(2);
      const unsigned sq2 = qseq++;
      if (c.wq == 0 && it > 0) lds_wait_ge4((volatile LAS v4u*)(fl + 32 + 4 * c.grp), (unsigned)it);
      q_arm<256>(c, qb_off + q_base(b, 1), sq2);
      MAIN(3); MAIN(4); MAIN(5); MAIN(6); MAIN(7); MAIN(8); MAIN(9);
      if (c.wq == 0) q_dma<256>(c, qb_off + q_base(b, 1));
      MAIN(10); MAIN(11);
      q_wait<256>(c, qb_off + q_base(b, 1), sq2);
      const v4u q2 = *(const LAS v4u*)(L + L_QB + c.grp * 1280 + 1024 + r * 16);
      SoftState st;
      b_chunk<MU(0), 8, true>(K, V, q2, ok0, st, r, mk);
      SCHED0();
      MAIN(12); MAIN(13);
      b_chunk<MU(4), 8, false>(K, V, q2, true, st, r, mk);
      SCHED0();
      MAIN(14); MAIN(15);
      b_chunk<MU(8), 8, false>(K, V, q2, true, st, r, mk);
      SCHED0();
      MAIN(16); MAIN(17);
      b_chunk<MU(12), 8, false>(K, V, q2, true, st, r, mk);
      SCHED0();
      MAIN(18);
      if (lane == 0) fl[c.wq] = (unsigned)(b + 1);
      b_chunk<MU(16), 4, false>(K, V, q2, true, st, r, mk);
      SCHED0();
      b_chunk<MU(18), 2, false>(K, V, q2, true, st, r, mk);
      b_publish(c, b, 1, st, (unsigned)(b + 1));
    }
    const unsigned sp = so - 2 * BATCH_B;
    {
      const unsigned sq = qseq++;
      q_arm<1024>(c, qa_off + q_base(b, 2), sq);
      if (it > 0) {
        LANE_CONSTS
        RRS(0, W0); RRS(1, W1);
        RR_STEP(0, NO - 1) RR_STEP(2, NO - 1) RR_STEP(4, NO - 1) RR_STEP(6, NO - 1)
      }
      q_wait<1024>(c, qa_off + q_base(b, 2), sq);
      const AScores sc = a_qk(c, c.ldsA);
      a_pv(c, sc, c.ldsA, c.ldsA);
      if (c.lane == 0) fl[16 + 4 * c.grp + c.wq] = (unsigned)(it + 1);
      lds_wait_ge4((volatile LAS v4u*)(fl + 16 + 4 * c.grp), (unsigned)(it + 1));
      a_merge<false>(c, b, 2);
      if (c.lane == 0) fl[24 + 4 * c.grp + c.wq] = (unsigned)(it + 1);
    }
    if (it < 3) {
      lds_wait_ge4((volatile LAS v4u*)(fl + 24 + 4 * c.grp), (unsigned)(it + 1));
      wait_vm<0>();
      if (b == 0 && c.wq == 0) lds_wait_ge(fl + 58, 1u);
      const unsigned sq = qseq++;
      q_arm<1024>(c, qa_off + q_base(b + 2, 0), sq);
      a_issue<THR_A>(p, c, b + 2, c.ldsA);
      wait_vm<0>();
      A_PASS1(b + 2, sq, c.ldsA)
    }
    {
      const unsigned sq = qseq++;
      q_arm<256>(c, qb_off + q_base(b, 3), sq);
      const int lane = opaque(c.lane), r = lane & 15;
      const v4u mk = pv_masks(lane);
      if (it > 0) {
        lds_wait_ge4((volatile LAS v4u*)(fl + 4), (unsigned)min(b + 3, 8));
        b_publish(c, b - 2, 3, st4, (unsigned)(b - 1), (unsigned)(L_SCR + 1536), 48, BOX);
      }
      q_wait<256>(c, qb_off + q_base(b, 3), sq);
      q4 = *(const LAS v4u*)(L + L_QB + c.grp * 1280 + 1024 + r * 16);
      wait_lgkm();
      if (lane == 0) fl[32 + 4 * c.grp + c.wq] = (unsigned)(it + 1);
      b_chunk<2 * NO, 2 * (NS - 2), true>(K, V, q4, true, st4, r, mk);
      SCHED0();
    }
  }
  {
    LANE_CONSTS
    LAS unsigned char* box = L + L_SCR + c.wq * BOX;
    if (c.grp == 0) {
#pragma unroll 1
      for (int b = 6; b < 8; ++b) {
        const unsigned sp = (unsigned)b * BATCH_B + b_off;
        RRS(0, 0); RRS(1, 1); RRS(2, 2); RRS(3, 3); RRS(4, 4); RRS(5, 5); RRS(6, 6);
        if (b == 7) {
          lds_wait_ge(fl + 36 + c.wq, 4u);
          q4 = *(const LAS v4u*)(L + L_QB + 1280 + 1024 + r * 16);
          st4.m = -INFINITY; st4.l = 0.f; st4.a = f32x4{0.f, 0.f, 0.f, 0.f};
        }
        b_chunk<0, 2 * NO, false>(K, V, q4, ok0, st4, r, mk);
        if (b == 6) b_publish(c, 6, 3, st4, 1u, 4096u, 52);
      }
      *(LAS f32x4*)(box + lane * 16) = st4.a;
      *(LAS f32x2*)(box + 1024 + lane * 8) = f32x2{st4.m, st4.l};
      wait_lgkm();
      if (lane == 0) fl[40 + c.wq] = 1u;
    } else {
      lds_wait_ge(fl + 40 + c.wq, 1u);
      const f32x4 a2 = *(const LAS f32x4*)(box + lane * 16);
      const f32x2 ml = *(const LAS f32x2*)(box + 1024 + lane * 8);
      const float mn = fmx(st4.m, ml.x), e1 = ex2((st4.m - mn) * LOG2E), e2 = ex2((ml.x - mn) * LOG2E);
      st4.a = st4.a * e1 + a2 * e2; st4.l = st4.l * e1 + ml.y * e2; st4.m = mn;
      b_publish(c, 7, 3, st4, 9u);
    }
  }
#undef POFF
#undef RRS
#undef RR_STEP
#undef LANE_CONSTS
#undef MAIN
#undef MU
#undef A_PASS1
}

struct WSlice { unsigned w[96]; };

__device__ __forceinline__ void pack_store(const v4u a, const v4u b, int n, WSlice& W, LAS unsigned char* lw) {
  const unsigned av[4] = {a.x, a.y, a.z, a.w}, bv[4] = {b.x, b.y, b.z, b.w};
  unsigned o[8];
#pragma unroll
  for (int i = 0; i < 4; ++i) {
    o[2 * i] = __builtin_amdgcn_perm(bv[i], av[i], 0x05040100u);
    o[2 * i + 1] = __builtin_amdgcn_perm(bv[i], av[i], 0x07060302u);
  }
  if (n < 12) {
#pragma unroll
    for (int e = 0; e < 8; ++e) W.w[8 * n + e] = (unsigned)opaque((int)o[e]);
  } else {
    *(LAS v4u*)(lw + (2 * (n - 12)) * 8192) = v4u{o[0], o[1], o[2], o[3]};
    *(LAS v4u*)(lw + (2 * (n - 12) + 1) * 8192) = v4u{o[4], o[5], o[6], o[7]};
  }
}
template <bool GQA, int DEPTH>
__device__ __forceinline__ void load_wo(const unsigned short* wo, int h0, int w, int lane, WSlice& W, LAS unsigned char* lw) {
  const rsrc_t rs = mkrs(wo, NH * DH * DM * 2);
  const unsigned base = (unsigned)(((h0 + (w >> 2)) * DH + 32 * (w & 3)) * (DM * 2));
  v4u raw[4][8];
#pragma unroll
  for (int st = 0; st < 4 + DEPTH - 1; ++st) {
    if (st < 4) {
      const int g = 3 - st;
#pragma unroll
      for (int pc = 0; pc < 4; ++pc)
#pragma unroll
        for (int e = 0; e < 2; ++e) {
          const int k = GQA ? 8 * (2 * e + (g >> 1)) + 4 * (g & 1) + pc : 8 * g + 2 * pc + e;
          raw[g][2 * pc + e] = ld_nt(rs, lane * 16, base + k * (DM * 2));
        }
      SCHED0();
    }
    if (st >= DEPTH - 1) {
      const int g = 3 - (st - (DEPTH - 1));
#pragma unroll
      for (int pc = 0; pc < 4; ++pc) pack_store(raw[g][2 * pc], raw[g][2 * pc + 1], 4 * g + pc, W, lw);
      SCHED0();
    }
  }
}
template <int DEPTH>
__device__ __forceinline__ void load_wq(const unsigned short* wq, int h0, int w, int lane, WSlice& W, LAS unsigned char* lw) {
  const rsrc_t rs = mkrs(wq, NH * DH * DM * 2);
  const unsigned voff = (unsigned)((lane >> 4) * (NH * DH * 2) + (lane & 15) * 16), base = (unsigned)(64 * w * (NH * DH * 2) + h0 * (DH * 2));
  v4u raw[4][8];
#pragma unroll
  for (int st = 0; st < 4 + DEPTH - 1; ++st) {
    if (st < 4) {
      const int q = 3 - st;
#pragma unroll
      for (int i = 0; i < 4; ++i)
#pragma unroll
        for (int e = 0; e < 2; ++e) {
          const int n = 4 * q + i, n2 = n >> 1, hd = n & 1;
          raw[q][2 * i + e] = ld_nt(rs, voff, base + 4 * (2 * n2 + e) * (NH * DH * 2) + hd * (DH * 2));
        }
      SCHED0();
    }
    if (st >= DEPTH - 1) {
      const int q = 3 - (st - (DEPTH - 1));
#pragma unroll
      for (int i = 0; i < 4; ++i) pack_store(raw[q][2 * i], raw[q][2 * i + 1], 4 * q + i, W, lw);
      SCHED0();
    }
  }
}

struct SvcCtx { rsrc_t rw; LAS unsigned char* L; int h0, j16, w, lane; const unsigned short* x0; unsigned short* out; };

__device__ __forceinline__ void svc_s1(const SvcCtx& c, const WSlice& W, int b, int layer) {
  const int lane = opaque(c.lane), w = c.w, sg = lane >> 2, pc = lane & 3, tid = w * 64 + lane;
  const unsigned rec = rec_base(b, layer, c.h0 + (w >> 2));
  const int nsrc = (layer & 1) ? NSRC_B : NSRC_A;
  const bool v0 = sg < nsrc, v1 = sg + 16 < nsrc;
  const unsigned o0 = sg * REC_B, po = (4 * (w & 3) + pc) * 16;
  typedef unsigned v3u __attribute__((ext_vector_type(3)));
  v4u d[2]; v3u e[2];
  d[0] = v0 ? v4u{SENT, SENT, SENT, SENT} : v4u{0u, 0u, 0u, 0u}; e[0] = v0 ? v3u{SENT, SENT, SENT} : v3u{0xFF800000u, 0u, 0u};
  d[1] = v1 ? v4u{SENT, SENT, SENT, SENT} : v4u{0u, 0u, 0u, 0u}; e[1] = v1 ? v3u{SENT, SENT, SENT} : v3u{0xFF800000u, 0u, 0u};
  for (unsigned spins = 0; spins < SPIN_CAP; ++spins) {
    CFENCE();
    if (__builtin_amdgcn_readfirstlane(__builtin_amdgcn_raw_buffer_load_b32(c.rw, 256 + 3 * REC_B, rec, 16)) != SENT) break;
  }
  for (unsigned spins = 0; spins < SPIN_CAP; ++spins) {
    CFENCE();
#pragma unroll
    for (int i = 0; i < 2; ++i) {
      if (max(max(e[i].x, e[i].y), e[i].z) == SENT) e[i] = __builtin_amdgcn_raw_buffer_load_b96(c.rw, o0 + i * 16 * REC_B + 256, rec, 16);
      if (umax4(d[i]) == SENT) d[i] = ld_sc1(c.rw, o0 + i * 16 * REC_B + po, rec);
    }
    SCHED0();
    const unsigned mx = max(max(umax4(d[0]), umax4(d[1])), max(max(max(e[0].x, e[0].y), e[0].z), max(max(e[1].x, e[1].y), e[1].z)));
    if (all_lanes(mx != SENT)) break;
  }
  const LAS unsigned char* lw = c.L + S_W + tid * 16;
  v4u xl[4];
#pragma unroll
  for (int i = 0; i < 4; ++i) xl[i] = *(const LAS v4u*)(lw + i * 8192);
  const float m0 = __uint_as_float(e[0].x), m1 = __uint_as_float(e[1].x);
  float M = fmx(m0, m1);
  M = fmx(M, dppf<ROR4>(M)); M = fmx(M, dppf<XOR8>(M)); M = xrow_max(M);
  const float w0 = ex2((m0 - M) * LOG2E), w1 = ex2((m1 - M) * LOG2E);
  float Ls = w0 * __uint_as_float(e[0].y) + w1 * __uint_as_float(e[1].y);
  const float f0 = w0 * __uint_as_float(e[0].z), f1 = w1 * __uint_as_float(e[1].z);
  float o[8];
#pragma unroll
  for (int i = 0; i < 4; ++i) {
    const unsigned u0 = d[0][i], u1 = d[1][i];
    const h16x2 a0 = __builtin_bit_cast(h16x2, u0), a1 = __builtin_bit_cast(h16x2, u1);
    o[2 * i] = (float)a0.x * f0 + (float)a1.x * f1;
    o[2 * i + 1] = (float)a0.y * f0 + (float)a1.y * f1;
  }
  float oa = rs4(o[0], o[1], o[2], o[3]), ob = rs4(o[4], o[5], o[6], o[7]);
  Ls = xrow_sum(Ls);
  oa += dppf<ROR4>(oa); ob += dppf<ROR4>(ob); Ls += dppf<ROR4>(Ls);
  oa += dppf<XOR8>(oa); ob += dppf<XOR8>(ob); Ls += dppf<XOR8>(Ls);
  const float inv = __builtin_amdgcn_rcpf(Ls);
  const unsigned pr = pk2(oa * inv, ob * inv);
  float y[8];
#pragma unroll
  for (int i = 0; i < 8; ++i) y[i] = 0.f;
#pragma unroll
  for (int n = 12; n < 14; ++n) {
    const unsigned op = __builtin_amdgcn_readlane(pr, 16 * (n >> 2) + (n & 3));
#pragma unroll
    for (int i = 0; i < 8; ++i) y[i] = dot2(xl[2 * (n - 12) + (i >> 2)][i & 3], op, y[i]);
  }
  SCHED0();
#pragma unroll
  for (int i = 0; i < 4; ++i) xl[i] = *(const LAS v4u*)(lw + (4 + i) * 8192);
#pragma unroll
  for (int n = 0; n < 12; ++n) {
    const unsigned op = __builtin_amdgcn_readlane(pr, 16 * (n >> 2) + (n & 3));
#pragma unroll
    for (int i = 0; i < 8; ++i) y[i] = dot2(W.w[8 * n + i], op, y[i]);
  }
#pragma unroll
  for (int n = 14; n < 16; ++n) {
    const unsigned op = __builtin_amdgcn_readlane(pr, 16 * (n >> 2) + (n & 3));
#pragma unroll
    for (int i = 0; i < 8; ++i) y[i] = dot2(xl[2 * (n - 14) + (i >> 2)][i & 3], op, y[i]);
  }
  LAS float* red = (LAS float*)(c.L + S_RED);
  *(LAS f32x4*)(red + w * DM + lane * 8) = f32x4{y[0], y[1], y[2], y[3]};
  *(LAS f32x4*)(red + w * DM + lane * 8 + 4) = f32x4{y[4], y[5], y[6], y[7]};
  bar();
  {
    const int g = lane >> 4, cq = lane & 15;
    const LAS float* rp = red + 2 * g * DM + 64 * w + 4 * cq;
    const f32x4 s = *(const LAS f32x4*)(rp) + *(const LAS f32x4*)(rp + DM);
    const float v = rs4(s[0], s[1], s[2], s[3]);
    st_sc1_32(c.rw, (unsigned)((64 * w + 4 * cq + g) * 4), y_base(b, layer) + (unsigned)(c.j16 * (DM * 4)), __float_as_uint(v));
  }
}

__device__ __forceinline__ unsigned x_poll(const SvcCtx& c, unsigned voff, unsigned xo, unsigned first) {
  unsigned v = first;
  for (unsigned spins = 0; !all_lanes(v != 0xFFFFu) && spins < SPIN_CAP; ++spins) {
    CFENCE();
    v = __builtin_amdgcn_raw_buffer_load_b16(c.rw, voff, xo, 16);
  }
  return v;
}

template <bool GATHER>
__device__ __forceinline__ void svc_s2(const SvcCtx& c, const WSlice& W, int b, int layer, unsigned xin = 0u, bool have_x = false) {
  const int lane = opaque(c.lane), w = c.w, g = lane >> 4, cq = lane & 15, tid = w * 64 + lane;
  const int col = 64 * w + 4 * cq + g;
  const unsigned xprev = x_base(b, layer - 1);
  unsigned xb;
  if (have_x) xb = xin;
  else if (!GATHER || layer == 0) xb = c.x0[b * DM + col];
  else xb = __builtin_amdgcn_raw_buffer_load_b16(c.rw, col * 2, xprev, 16);
  float xv;
  if constexpr (GATHER) {
    const unsigned yo = y_base(b, layer);
    const unsigned vo = (unsigned)(4 * g * (DM * 4) + (64 * w + 4 * cq) * 4);
    v4u d[4];
#pragma unroll
    for (int i = 0; i < 4; ++i) d[i] = v4u{SENT, SENT, SENT, SENT};
    for (unsigned spins = 0; spins < SPIN_CAP; ++spins) {
      CFENCE();
#pragma unroll
      for (int i = 0; i < 4; ++i)
        if (umax4(d[i]) == SENT) d[i] = ld_sc1(c.rw, vo + i * (DM * 4), yo);
      SCHED0();
      if (all_lanes(max(max(umax4(d[0]), umax4(d[1])), max(umax4(d[2]), umax4(d[3]))) != SENT)) break;
    }
    if (layer > 0) xb = x_poll(c, col * 2, xprev, xb);
    const f32x4 s = (__builtin_bit_cast(f32x4, d[0]) + __builtin_bit_cast(f32x4, d[1])) + (__builtin_bit_cast(f32x4, d[2]) + __builtin_bit_cast(f32x4, d[3]));
    xb = f2bf(bf2f(xb) + rbf(rs4(s[0], s[1], s[2], s[3])));
    if (c.j16 == 0) {
      __builtin_amdgcn_raw_buffer_store_b16((unsigned short)min(xb, 0xFFFEu), c.rw, col * 2, x_base(b, layer), 16);
    }
  }
  xv = bf2f(xb);
  CFENCE();
  const LAS unsigned char* lw = c.L + S_W + 65536 + tid * 16;
  v4u xl[4];
#pragma unroll
  for (int i = 0; i < 4; ++i) xl[i] = *(const LAS v4u*)(lw + i * 8192);
  const float piv = __builtin_bit_cast(float, __builtin_amdgcn_readfirstlane(__builtin_bit_cast(int, xv)));
  const float dv = xv - piv;
  const float s1 = xrow_sum(row_sum(dv)), s2 = xrow_sum(row_sum(dv * dv));
  LAS float* stt = (LAS float*)(c.L + S_ST);
  if (lane == 0) *(LAS f32x2*)(stt + 2 * w) = f32x2{piv + s1 * (1.f / 64.f), s2 - s1 * s1 * (1.f / 64.f)};
  bar();
  float mean, M2;
  {
    const f32x2 t = *(const LAS f32x2*)(stt + 2 * (lane & 7));
    float ms = t.x;
    ms += dppf<XOR1>(ms); ms += dppf<XOR2>(ms); ms += dppf<XOR7>(ms);
    mean = ms * 0.125f;
    const float dd = t.x - mean;
    M2 = fmaf(64.f * dd, dd, t.y);
    M2 += dppf<XOR1>(M2); M2 += dppf<XOR2>(M2); M2 += dppf<XOR7>(M2);
  }
  const float mb = rbf(mean), den = rbf(__builtin_amdgcn_sqrtf(rbf(rbf(fmx(M2, 0.f) * (1.f / DM)) + 1e-5f)));
  const float xn = rbf(xv - mb) * __builtin_amdgcn_rcpf(den);
  const unsigned pr = pk2(xn, dppf<XOR1>(xn));
  float y[2][8];
#pragma unroll
  for (int i = 0; i < 8; ++i) { y[0][i] = 0.f; y[1][i] = 0.f; }
#define QSTEP_L(N2, BASE)                                                                                   \
  {                                                                                                         \
    const unsigned xp = dppu<BCAST + 2 * (N2)>(pr);                                                         \
    _Pragma("unroll") for (int i = 0; i < 8; ++i) {                                                         \
      y[0][i] = dot2(xl[(i >> 2)][i & 3], xp, y[0][i]);                                                     \
      y[1][i] = dot2(xl[2 + (i >> 2)][i & 3], xp, y[1][i]);                                                 \
    }                                                                                                       \
  }
#define QSTEP_R(N2)                                                                                         \
  {                                                                                                         \
    const unsigned xp = dppu<BCAST + 2 * (N2)>(pr);                                                         \
    _Pragma("unroll") for (int i = 0; i < 8; ++i) {                                                         \
      y[0][i] = dot2(W.w[16 * (N2) + i], xp, y[0][i]);                                                      \
      y[1][i] = dot2(W.w[16 * (N2) + 8 + i], xp, y[1][i]);                                                  \
    }                                                                                                       \
  }
  QSTEP_L(6, 0)
  SCHED0();
#pragma unroll
  for (int i = 0; i < 4; ++i) xl[i] = *(const LAS v4u*)(lw + (4 + i) * 8192);
  QSTEP_R(0) QSTEP_R(1) QSTEP_R(2) QSTEP_R(3) QSTEP_R(4) QSTEP_R(5)
  QSTEP_L(7, 0)
#undef QSTEP_L
#undef QSTEP_R
  LAS float* red = (LAS float*)(c.L + S_RED2);
#pragma unroll
  for (int hd = 0; hd < 2; ++hd) {
    red[w * 256 + hd * DH + cq * 8 + g] = rs4(y[hd][0], y[hd][1], y[hd][2], y[hd][3]);
    red[w * 256 + hd * DH + cq * 8 + 4 + g] = rs4(y[hd][4], y[hd][5], y[hd][6], y[hd][7]);
  }
  bar();
  if (w < 2) {
    f32x2 t[8];
#pragma unroll
    for (int i = 0; i < 8; ++i) t[i] = *(const LAS f32x2*)(red + i * 256 + tid * 2);
    const f32x2 s = ((t[0] + t[1]) + (t[2] + t[3])) + ((t[4] + t[5]) + (t[6] + t[7]));
    st_sc1_32(c.rw, tid * 4, q_base(b, layer + 1) + (unsigned)(c.h0 * (DH * 2)), pk2(s.x, s.y));
  }
}

__device__ __forceinline__ void svc_final(const SvcCtx& c) {
  const int lane = opaque(c.lane), b = c.w, pj = lane >> 3, cq = lane & 7;
  const unsigned yo = y_base(b, 3);
  const unsigned vo = (unsigned)(pj * (DM * 4) + (32 * c.j16 + 4 * cq) * 4);
  const int col = 32 * c.j16 + 4 * cq + (lane >> 4);
  const unsigned xb = x_poll(c, col * 2, x_base(b, 2), 0xFFFFu);
  v4u d0, d1;
  for (unsigned spins = 0;; ++spins) {
    CFENCE();
    d0 = ld_sc1(c.rw, vo, yo); d1 = ld_sc1(c.rw, vo + 8 * (DM * 4), yo);
    if (all_lanes(max(umax4(d0), umax4(d1)) != SENT) || spins > SPIN_CAP) break;
  }
  const f32x4 s = __builtin_bit_cast(f32x4, d0) + __builtin_bit_cast(f32x4, d1);
  float v = rs4(s[0], s[1], s[2], s[3]);
  v += dppf<XOR8>(v);
  if ((lane & 8) == 0) c.out[b * DM + col] = (unsigned short)f2bf(bf2f(xb) + rbf(v));
}

__device__ __forceinline__ void service_role(const Params& p, LAS unsigned char* L, int x, int slot, int w) {
  SvcCtx c;
  c.lane = opaque(lane_id()); c.w = w;
  const int tid = w * 64 + c.lane, set = slot >> 1;
  c.rw = mkrs(p.ws, WS_END); c.L = L; c.h0 = 4 * x + 2 * (slot & 1); c.j16 = 2 * x + (slot & 1); c.x0 = p.x0; c.out = p.out;
  const unsigned tag = launch_tag(c.rw);
  WSlice Wo, Wq;
  LAS unsigned char* lw = L + S_W + tid * 16;
  if (set == 0) {
    const int col = 64 * w + 4 * (c.lane & 15) + (c.lane >> 4);
    const unsigned xa = p.x0[col], xb = p.x0[DM + col];
    load_wq<4>(p.wqA, c.h0, w, c.lane, Wq, lw + 65536);
    armed_wait(c.rw, tag, 0u, c.lane);
#pragma unroll 1
    for (int b = 0; b < 2; ++b) svc_s2<false>(c, Wq, b, -1, b ? xb : xa, true);
    load_wo<false, 3>(p.woB, c.h0, w, c.lane, Wo, lw);
    armed_wait(c.rw, tag, 1u, c.lane);
  } else {
    load_wo<true, 4>(p.woA, c.h0, w, c.lane, Wo, lw);
    load_wq<3>(p.wqB, c.h0, w, c.lane, Wq, lw + 65536);
    armed_wait(c.rw, tag, 0u, c.lane);
  }
  for (int i = 0; i < 2 * NB; ++i) {
    int b, second;
    if (i < 2 + (set == 0)) { b = i; second = 0; }
    else if (i >= 2 * NB - 2 - (set == 0)) { b = i - NB; second = 1; }
    else if (set == 0) { second = !(i & 1); b = second ? (i - 4) >> 1 : (i + 3) >> 1; }
    else { second = !(i & 1); b = second ? (i - 2) >> 1 : (i + 1) >> 1; }
    const int layer = 2 * second + 1 - set;
    if (set == 1 && i == 2) armed_wait(c.rw, tag, 1u, opaque(c.lane));
    svc_s1(c, Wo, b, layer);
    if (layer < 3) svc_s2<true>(c, Wq, b, layer);
    else bar();
    if (set == 0 && layer == 1 && b + 2 < NB) svc_s2<false>(c, Wq, b + 2, -1);
  }
  if (set == 0) svc_final(c);
  if (blockIdx.x == 0 && tid == 7 * 64) __builtin_amdgcn_raw_buffer_store_b32(tag, c.rw, 0, WS_EPOCH, 16);
}

__global__ void __launch_bounds__(NTHR, 2) k_mega(Params p) {
  extern __shared__ __attribute__((aligned(16))) unsigned char smem[];
  LAS unsigned char* L = (LAS unsigned char*)smem;
  const int x = blockIdx.x & 7, slot = blockIdx.x >> 3, w = __builtin_amdgcn_readfirstlane(threadIdx.x >> 6);
  if (slot < 4) service_role(p, L, x, slot, w);
  else stream_role(p, L, x, slot - 4, w);
}
}

extern "C" void kernel_launch(void* const* d_in, const int* in_sizes, int n_in, void* d_out, int out_size, void* d_ws, size_t ws_size, hipStream_t stream) {
  using namespace mk;
  Params p;
  p.x0 = (const unsigned short*)d_in[0];
  p.kA = (const unsigned short*)d_in[1]; p.vA = (const unsigned short*)d_in[2]; p.wqA = (const unsigned short*)d_in[3]; p.woA = (const unsigned short*)d_in[4];
  p.kB = (const unsigned short*)d_in[6]; p.vB = (const unsigned short*)d_in[7]; p.wqB = (const unsigned short*)d_in[8]; p.woB = (const unsigned short*)d_in[9];
  p.out = (unsigned short*)d_out;
  p.ws = (unsigned char*)d_ws;
  (void)hipFuncSetAttribute((const void*)k_mega, hipFuncAttributeMaxDynamicSharedMemorySize, LDS_BYTES);
  k_mega<<<NWG, NTHR, LDS_BYTES, stream>>>(p);
}
```
